```python
import torch

def _softmax(x, axis=-1):
    return torch.softmax(x, dim=axis)
def _gelu(x):
    return torch.nn.functional.gelu(x, approximate="tanh")
def _silu(x):
    return torch.nn.functional.silu(x)
def _sigmoid(x):
    return torch.sigmoid(x)
def _layernorm(x, axis=-1, eps=1e-5):
    m = x.mean(dim=axis, keepdim=True)
    v = x.var(dim=axis, keepdim=True, unbiased=False)
    return (x - m) / torch.sqrt(v + eps)
def _scatter_add(tgt, idx, src, axis=0):
    return tgt.clone().index_add_(axis, idx, src)
def _segment_sum(src, seg, n):
    out = torch.zeros((n,) + src.shape[1:], dtype=src.dtype)
    return out.index_add_(0, seg, src)
def _pad(x, pw):
    flat = tuple(v for pair in reversed(pw) for v in pair)
    return torch.nn.functional.pad(x, flat)

kernel_name = 'sequential_preLN__ssm_conv_s4d__moe_arch_mod__prompt'

def setup_inputs():

    _w = (lambda a: (lambda t: t.to(torch.bfloat16) if t.is_floating_point() else t)(torch.as_tensor(a)))
    def _A_setup():
        Bsz, L, D, N = 1, 2048, 640, 128
        return {
            "u": _w((lambda *s: torch.randn(*s, dtype=torch.bfloat16) * 0.05)(Bsz, L, D)),
            "log_A_real": _w((lambda *s: torch.randn(*s, dtype=torch.bfloat16) * 6.4)(D, N) * 0.5),
            "A_imag": _w((lambda *s: torch.randn(*s, dtype=torch.bfloat16) * 0.05)(D, N) * 3.14),
            "C_real": _w((lambda *s: torch.randn(*s, dtype=torch.bfloat16) * 0.05)(D, N) / N ** 0.5),
            "C_imag": _w((lambda *s: torch.randn(*s, dtype=torch.bfloat16) * 0.05)(D, N) / N ** 0.5),
            "log_dt": _w((lambda *s: torch.randn(*s, dtype=torch.bfloat16) * 0.05)(D) * 0.5 - 4.0),
            "D_skip": _w((lambda *s: torch.randn(*s, dtype=torch.bfloat16) * 0.05)(D)),
        }
    _A_in = _A_setup()

    def _B_setup():
        B, S, D, F, C = 1, 2048, 640, 1728, 1
        return {
            "x": _w((lambda *s: torch.randn(*s, dtype=torch.bfloat16) * 0.05)(B, S, D)),
            "w_r": _w((lambda *s: torch.randn(*s, dtype=torch.bfloat16) * 0.05)(D)),
            "w_gate": _w((lambda *s: torch.randn(*s, dtype=torch.bfloat16) * 0.05)(D, F)),
            "w_up": _w((lambda *s: torch.randn(*s, dtype=torch.bfloat16) * 0.05)(D, F)),
            "w_down": _w((lambda *s: torch.randn(*s, dtype=torch.bfloat16) * 0.05)(F, D)),
        }
    _B_in = _B_setup()

    return {
        "A_u": _A_in["u"],
        "A_log_A_real": _A_in["log_A_real"],
        "A_A_imag": _A_in["A_imag"],
        "A_C_real": _A_in["C_real"],
        "A_C_imag": _A_in["C_imag"],
        "A_log_dt": _A_in["log_dt"],
        "A_D_skip": _A_in["D_skip"],
        "B_x": _B_in["x"],
        "B_w_r": _B_in["w_r"],
        "B_w_gate": _B_in["w_gate"],
        "B_w_up": _B_in["w_up"],
        "B_w_down": _B_in["w_down"],
    }

def reference(A_u, A_log_A_real, A_A_imag, A_C_real, A_C_imag, A_log_dt, A_D_skip, B_x, B_w_r, B_w_gate, B_w_up, B_w_down):
    def _A_ref(u, log_A_real, A_imag, C_real, C_imag, log_dt, D_skip):
        L = u.shape[1]
        dt = torch.exp(log_dt)
        A = -torch.exp(log_A_real) + 1j * A_imag
        C = (C_real + 1j * C_imag) * dt[:, None]
        pos = (lambda x, dt: x.to(dt))(torch.arange(L), log_dt.dtype)
        P = torch.exp((A * dt[:, None])[:, :, None] * pos[None, None, :])
        K = 2.0 * torch.einsum("dn,dnl->dl", C, P).real
        L2 = 2 * L
        u_t = (lambda x, *d: x.permute(*d))(u, 0, 2, 1)
        Kf = (lambda x, *a, **k: torch.fft.rfft(x.float(), *a, **k))(K, L2)
        Uf = (lambda x, *a, **k: torch.fft.rfft(x.float(), *a, **k))(u_t, L2)
        y = (lambda x, *a, **k: torch.fft.irfft(x, *a, **k).to(torch.bfloat16))(Uf * Kf[None, :, :], L2)[:, :, :L]
        y = (lambda x, *d: x.permute(*d))(y, 0, 2, 1)
        return y + u * D_skip[None, None, :]

    def _B_ref(x, w_r, w_gate, w_up, w_down):
        B, S, D = x.shape
        C = 1
        r = x @ w_r
        idx = torch.argsort(-r, axis=-1)[:, :C]
        idx_e = (lambda x, n, axis: torch.repeat_interleave(x, n, dim=axis))(idx.reshape(B, C, 1), D, axis=2)
        x_sel = (lambda x, i, axis: torch.take_along_dim(x, i, dim=axis))(x, idx_e, axis=1)
        g = _silu(x_sel @ w_gate)
        u = x_sel @ w_up
        y = (g * u) @ w_down
        r_sel = _sigmoid((lambda x, i, axis: torch.take_along_dim(x, i, dim=axis))(r, idx, axis=1))
        y = y * r_sel.reshape(B, C, 1)
        b_off = (torch.arange(B) * S).reshape(B, 1)
        flat_idx = (idx + b_off).reshape(B * C)
        zero = (x * 0.0).reshape(B * S, D)
        upd = _scatter_add(zero, flat_idx, y.reshape(B * C, D), axis=0)
        return x + upd.reshape(B, S, D)

    x = A_u
    h = x + _A_ref(_layernorm(x, axis=-1), A_log_A_real, A_A_imag, A_C_real, A_C_imag, A_log_dt, A_D_skip)
    h2 = h + _B_ref(_layernorm(h, axis=-1), B_w_r, B_w_gate, B_w_up, B_w_down)
    return h2
```

```cpp
#include <hip/hip_runtime.h>
#include <hip/hip_bf16.h>
#include <stdint.h>

typedef _Float16 f16;
typedef f16 f16x2 __attribute__((ext_vector_type(2)));
typedef f16 f16x4 __attribute__((ext_vector_type(4)));
typedef f16 f16x8 __attribute__((ext_vector_type(8)));
typedef float f32x4 __attribute__((ext_vector_type(4)));
typedef float f32x16 __attribute__((ext_vector_type(16)));
typedef unsigned u32x2 __attribute__((ext_vector_type(2)));
typedef unsigned u32x4 __attribute__((ext_vector_type(4)));
typedef unsigned short u16;

constexpr int L = 2048, D = 640, NM = 128, F = 1728;

__device__ __forceinline__ float bf2f(unsigned x) { return __uint_as_float(x << 16); }
__device__ __forceinline__ float bflo(unsigned x) { return __uint_as_float(x << 16); }
__device__ __forceinline__ float bfhi(unsigned x) { return __uint_as_float(x & 0xffff0000u); }
__device__ __forceinline__ float rbf(float x) { return (float)(__bf16)x; }
__device__ __forceinline__ unsigned f2bf(float x) { __bf16 b = (__bf16)x; return (unsigned)__builtin_bit_cast(u16, b); }
__device__ __forceinline__ unsigned pkbf(float lo, float hi) { return f2bf(lo) | (f2bf(hi) << 16); }
__device__ __forceinline__ unsigned pkh(float lo, float hi) {
    f16x2 v; v.x = (f16)lo; v.y = (f16)hi; return __builtin_bit_cast(unsigned, v);
}

template <int CTRL>
__device__ __forceinline__ float dppf(float v) {
    return __builtin_bit_cast(float, __builtin_amdgcn_update_dpp(0, __builtin_bit_cast(int, v), CTRL, 0xf, 0xf, true));
}
template <int CTRL>
__device__ __forceinline__ unsigned dppu(unsigned v) {
    return (unsigned)__builtin_amdgcn_update_dpp(0, (int)v, CTRL, 0xf, 0xf, true);
}
__device__ __forceinline__ float wave_sum(float v) {
    v += dppf<0xB1>(v);
    v += dppf<0x4E>(v);
    v += dppf<0x141>(v);
    v += dppf<0x140>(v);
    {
        auto r = __builtin_amdgcn_permlane16_swap(__float_as_uint(v), __float_as_uint(v), false, false);
        v = __uint_as_float(r[0]) + __uint_as_float(r[1]);
    }
    {
        auto r = __builtin_amdgcn_permlane32_swap(__float_as_uint(v), __float_as_uint(v), false, false);
        v = __uint_as_float(r[0]) + __uint_as_float(r[1]);
    }
    return v;
}
__device__ __forceinline__ unsigned wave_max_u32(unsigned v) {
    v = max(v, dppu<0xB1>(v));
    v = max(v, dppu<0x4E>(v));
    v = max(v, dppu<0x141>(v));
    v = max(v, dppu<0x140>(v));
    {
        auto r = __builtin_amdgcn_permlane16_swap(v, v, false, false);
        v = max((unsigned)r[0], (unsigned)r[1]);
    }
    {
        auto r = __builtin_amdgcn_permlane32_swap(v, v, false, false);
        v = max((unsigned)r[0], (unsigned)r[1]);
    }
    return v;
}

struct cplx { float r, i; };
__device__ __forceinline__ cplx cmul(cplx a, cplx b) {
    cplx o; o.r = fmaf(a.r, b.r, -a.i * b.i); o.i = fmaf(a.r, b.i, a.i * b.r); return o;
}
__device__ __forceinline__ cplx csq(cplx a) {
    cplx o; o.r = fmaf(a.r, a.r, -a.i * a.i); o.i = 2.f * a.r * a.i; return o;
}
__device__ __forceinline__ cplx cexp_p(float zr, float zi, float p) {
    const float xr = zr * p, xi = zi * p;
    const float mag = __builtin_amdgcn_exp2f(xr * 1.44269504088896341f);
    const float c_hi = 0.15915494309189535f;
    const float c_lo = 6.4206383e-09f;
    float t = xi * c_hi;
    float e = fmaf(xi, c_hi, -t);
    e = fmaf(xi, c_lo, e);
    t = t - rintf(t);
    t = t + e;
    cplx o;
    o.r = mag * __builtin_amdgcn_cosf(t);
    o.i = mag * __builtin_amdgcn_sinf(t);
    return o;
}

__device__ __forceinline__ void row_stats8(const float (&a)[8], const float (&b)[8], float* red  ,
                                           int wave, int lane, float (&mean_b)[8], float (&sd_b)[8]) {
    float mw[8], m2[8];
#pragma unroll
    for (int t = 0; t < 8; ++t) mw[t] = wave_sum(a[t] + b[t]) * (1.f / 128.f);
#pragma unroll
    for (int t = 0; t < 8; ++t) {
        float da = a[t] - mw[t], db = b[t] - mw[t];
        m2[t] = wave_sum(fmaf(da, da, db * db));
    }
    if (lane == 0) {
#pragma unroll
        for (int t = 0; t < 8; ++t) { red[wave * 16 + t] = mw[t]; red[wave * 16 + 8 + t] = m2[t]; }
    }
    __syncthreads();
#pragma unroll
    for (int t = 0; t < 8; ++t) {
        float m0 = red[t], m1 = red[16 + t], m2_ = red[32 + t], m3 = red[48 + t], m4 = red[64 + t];
        float mean = (m0 + m1 + m2_ + m3 + m4) * 0.2f;
        float s = red[8 + t] + red[24 + t] + red[40 + t] + red[56 + t] + red[72 + t];
        float d0 = m0 - mean, d1 = m1 - mean, d2 = m2_ - mean, d3 = m3 - mean, d4 = m4 - mean;
        float q = d0 * d0 + d1 * d1 + d2 * d2 + d3 * d3 + d4 * d4;
        float var = (s + 128.f * q) * (1.f / 640.f);
        mean_b[t] = rbf(mean);
        sd_b[t] = rbf(sqrtf(rbf(rbf(var) + 1e-5f)));
    }
}

constexpr int ATAB_STRIDE = 132;
__global__ void __launch_bounds__(320) k1_ln_kgen(const u16* __restrict__ x, const u16* __restrict__ logA,
                                                  const u16* __restrict__ Aim, const u16* __restrict__ Cre,
                                                  const u16* __restrict__ Cim, const u16* __restrict__ logdt,
                                                  u32x4* __restrict__ uT, u32x4* __restrict__ Krev,
                                                  float* __restrict__ kscale) {
    __shared__ __attribute__((aligned(16))) unsigned smem[64 * ATAB_STRIDE + 704];
    const int tid = threadIdx.x, lane = tid & 63, wave = tid >> 6;
    if (blockIdx.x < 256) {
        const int blk = blockIdx.x;
        const unsigned* xr = (const unsigned*)(x + (size_t)blk * 8 * D) + tid;
        float a[8], b[8];
#pragma unroll
        for (int t = 0; t < 8; ++t) { unsigned v = xr[t * (D / 2)]; a[t] = bflo(v); b[t] = bfhi(v); }
        float mb[8], sb[8];
        row_stats8(a, b, (float*)smem, wave, lane, mb, sb);
        f16x8 ua, ub;
#pragma unroll
        for (int t = 0; t < 8; ++t) {
            ua[t] = (f16)rbf(rbf(a[t] - mb[t]) / sb[t]);
            ub[t] = (f16)rbf(rbf(b[t] - mb[t]) / sb[t]);
        }
        uT[(size_t)blk * D + 2 * tid] = __builtin_bit_cast(u32x4, ua);
        uT[(size_t)blk * D + 2 * tid + 1] = __builtin_bit_cast(u32x4, ub);
        return;
    }
    const int d = blockIdx.x - 256;
    unsigned* Atab = smem;
    unsigned* Btab = smem + 48 * ATAB_STRIDE;
    float* Tlin = (float*)(smem + 64 * ATAB_STRIDE);
    const float dt = rbf(expf(bf2f(logdt[d])));
    float cscale = 1.f;
    if (tid < 256) {
        const int n = tid & 127;
        const float zr = -rbf(expf(bf2f(logA[d * NM + n]))) * dt;
        const float zi = bf2f(Aim[d * NM + n]) * dt;
        const cplx e128 = cexp_p(zr, zi, 128.f);
        const cplx e256 = csq(e128), e512 = csq(e256);
        const cplx e384 = cmul(e128, e256);
#define PUTA(row, v) Atab[(row) * ATAB_STRIDE + n] = pkh((v).r, -(v).i)
#define PUTB(row, v) Btab[(row) * ATAB_STRIDE + n] = pkh((v).r, (v).i)
        if (tid >= 128) {
            cplx e[8];
            e[0].r = 1.f; e[0].i = 0.f;
            e[1] = cexp_p(zr, zi, 1.f);
            e[2] = csq(e[1]); e[4] = csq(e[2]);
            e[3] = cmul(e[1], e[2]); e[5] = cmul(e[1], e[4]); e[6] = cmul(e[2], e[4]); e[7] = cmul(e[3], e[4]);
#pragma unroll
            for (int r = 0; r < 8; ++r) { PUTA(r, e[r]); cplx v = cmul(e[r], e128); PUTA(8 + r, v); }
#pragma unroll
            for (int r = 0; r < 4; ++r) {
                cplx v = cmul(e[2 * r], e256); PUTA(16 + r, v);
                cplx w = cmul(e[2 * r], e384); PUTA(20 + r, w);
            }
            cplx s4[4]; s4[0] = e512; s4[1] = cmul(e512, e128); s4[2] = cmul(e512, e256); s4[3] = cmul(e512, e384);
#pragma unroll
            for (int s = 0; s < 4; ++s) { PUTA(24 + 2 * s, s4[s]); cplx v = cmul(s4[s], e[4]); PUTA(25 + 2 * s, v); }
        } else {
            const unsigned c0 = Cre[d * NM + lane] & 0x7fffu, c1 = Cre[d * NM + 64 + lane] & 0x7fffu;
            const unsigned c2 = Cim[d * NM + lane] & 0x7fffu, c3 = Cim[d * NM + 64 + lane] & 0x7fffu;
            unsigned mx = wave_max_u32(max(max(c0, c1), max(c2, c3)));
            int ex = (int)(mx >> 7);
            ex = min(max(ex, 8), 246);
            const float up = __uint_as_float((unsigned)(127 + 127 + 6 - ex) << 23);
            cscale = __uint_as_float((unsigned)(ex - 6) << 23);
            cplx c; c.r = bf2f(Cre[d * NM + n]) * up; c.i = bf2f(Cim[d * NM + n]) * up;
            const cplx e8 = cexp_p(zr, zi, 8.f);
            const cplx e16 = csq(e8), e32 = csq(e16), e64 = csq(e32);
            cplx b[8];
            b[0] = c; b[1] = cmul(c, e8); b[2] = cmul(c, e16); b[3] = cmul(b[1], e16);
#pragma unroll
            for (int j = 0; j < 4; ++j) b[4 + j] = cmul(b[j], e32);
#pragma unroll
            for (int j = 0; j < 8; ++j) { PUTB(j, b[j]); cplx v = cmul(b[j], e64); PUTB(8 + j, v); }
            const cplx e1024 = csq(e512), e2048 = csq(e1024);
            cplx s4[4]; s4[0] = e1024; s4[1] = cmul(e1024, e128); s4[2] = cmul(e1024, e256); s4[3] = cmul(e1024, e384);
#pragma unroll
            for (int s = 0; s < 4; ++s) { PUTA(32 + s, s4[s]); cplx v = cmul(s4[s], e512); PUTA(36 + s, v); }
            PUTA(40, e2048);
            if (tid == 0) kscale[d] = 2.f * dt * cscale;
        }
    }
    __syncthreads();
    if (wave < 3) {
        f32x4 acc = {0.f, 0.f, 0.f, 0.f};
        const int rr = lane & 15, q = lane >> 4;
        const unsigned* ap = Atab + (wave * 16 + rr) * ATAB_STRIDE + 4 * q;
        const unsigned* bp = Btab + rr * ATAB_STRIDE + 4 * q;
#pragma unroll
        for (int k = 0; k < 8; ++k) {
            f16x8 av = *(const f16x8*)(ap + 16 * k);
            f16x8 bv = *(const f16x8*)(bp + 16 * k);
            acc = __builtin_amdgcn_mfma_f32_16x16x32_f16(av, bv, acc, 0, 0, 0);
        }
#pragma unroll
        for (int reg = 0; reg < 4; ++reg) {
            const int c = wave * 16 + 4 * q + reg, j = rr;
            int idx;
            if (c < 16) idx = (c & 7) + 128 * (c >> 3) + 8 * j;
            else if (c < 24) idx = 256 + ((c - 16) & 3) + 64 * ((c - 16) >> 2) + 4 * j;
            else if (c < 32) idx = 384 + ((c - 24) & 1) + 32 * ((c - 24) >> 1) + 2 * j;
            else idx = 512 + 16 * (c - 32) + j;
            if (idx <= 640) Tlin[idx] = acc[reg];
        }
    }
    __syncthreads();
    if (tid < 256) {
        f16x8 o;
#pragma unroll
        for (int e = 0; e < 8; ++e) {
            const int l = 2047 - (8 * tid + e);
            const int idx = l < 128 ? l : (int)f2bf((float)l) - 0x4300 + 128;
            o[e] = (f16)Tlin[idx];
        }
        Krev[(size_t)d * 256 + tid] = __builtin_bit_cast(u32x4, o);
    }
}

constexpr int WAVE_LDS = 28160;
template <int TILE>
__device__ __forceinline__ void conv_stage(char* lds, const u32x4* __restrict__ uT, const u32x4* __restrict__ Krev,
                                           int d, int lane) {
    constexpr int NCH = TILE ? 4 : 2;
    constexpr int CSTR = TILE ? 4288 : 2240;
    constexpr int UOFF = 4 * CSTR;
    constexpr int KN = TILE ? 2048 : 1024;
    u32x4 uv[NCH], kv[NCH]; u32x2 kn[NCH];
    const u32x4* kb = Krev + (size_t)d * 256 + (TILE ? 0 : 128);
#pragma unroll
    for (int i = 0; i < NCH; ++i) {
        const int ch = i * 64 + lane;
        uv[i] = uT[(size_t)ch * D + d];
        kv[i] = kb[ch];
        if (ch + 1 < NCH * 64) kn[i] = *(const u32x2*)(kb + ch + 1); else kn[i] = (u32x2){0u, 0u};
    }
    const u32x4 z = {0u, 0u, 0u, 0u};
    *(u32x4*)(lds + UOFF + lane * 16) = z;
    *(u32x4*)(lds + UOFF + 1024 + lane * 16) = z;
    if (lane < 32) *(u32x4*)(lds + UOFF + 2048 + lane * 16) = z;
    if (lane < 32) {
        const int s = lane >> 3, w = lane & 7;
        if (w < 1) *(u32x4*)(lds + s * CSTR) = z;
        else if (w < 5) *(u32x4*)(lds + s * CSTR + (16 + KN) * 2 + (w - 1) * 16) = z;
    }
#pragma unroll
    for (int i = 0; i < NCH; ++i) {
        const int ch = i * 64 + lane;
        *(u32x4*)(lds + UOFF + 2560 + (ch >> 2) * 80 + (ch & 3) * 16) = uv[i];
        const unsigned w0 = kv[i].x, w1 = kv[i].y, w2 = kv[i].z, w3 = kv[i].w, w4 = kn[i].x, w5 = kn[i].y;
        const unsigned a01 = __builtin_amdgcn_alignbit(w1, w0, 16), a12 = __builtin_amdgcn_alignbit(w2, w1, 16),
                       a23 = __builtin_amdgcn_alignbit(w3, w2, 16), a34 = __builtin_amdgcn_alignbit(w4, w3, 16),
                       a45 = __builtin_amdgcn_alignbit(w5, w4, 16);
        char* p = lds + 32 + ch * 16;
        *(u32x4*)(p) = kv[i];
        *(u32x4*)(p + CSTR) = (u32x4){a01, a12, a23, a34};
        *(u32x4*)(p + 2 * CSTR) = (u32x4){w1, w2, w3, w4};
        *(u32x4*)(p + 3 * CSTR) = (u32x4){a12, a23, a34, a45};
        if (i == 0 && lane == 0) {
            *(u32x4*)(lds + 16) = z;
            *(u32x4*)(lds + 16 + CSTR) = (u32x4){0u, 0u, 0u, w0 << 16};
            *(u32x4*)(lds + 16 + 2 * CSTR) = (u32x4){0u, 0u, 0u, w0};
            *(u32x4*)(lds + 16 + 3 * CSTR) = (u32x4){0u, 0u, w0 << 16, a01};
        }
    }
}

template <int TILE>
__device__ __forceinline__ void conv_run(const char* lds, u16* __restrict__ y, const float* __restrict__ kscale,
                                         int d, int lane) {
    constexpr int CSTR = TILE ? 4288 : 2240;
    constexpr int UOFF = 4 * CSTR;
    constexpr int KN = TILE ? 2048 : 1024;
    constexpr int NMAX = KN / 16 - 1;
    const int r = lane & 31, h = lane >> 5;
    const int sg = (3 - r) & 3;
    const char* ap = lds + sg * CSTR + 2 * (KN + 15 - r - sg - 16 * NMAX + 8 * h);
    const char* bp = lds + UOFF + 2560 + (32 * TILE + r - (NMAX + 1) / 2) * 80 + 16 * h;
    f32x16 acc;
#pragma unroll
    for (int i = 0; i < 16; ++i) acc[i] = 0.f;
#pragma unroll 8
    for (int it = 0; it < (NMAX + 1) / 2; ++it) {
        f16x4 a0 = *(const f16x4*)(ap), a1 = *(const f16x4*)(ap + 8);
        f16x4 a2 = *(const f16x4*)(ap + 32), a3 = *(const f16x4*)(ap + 40);
        f16x8 b0 = *(const f16x8*)(bp + 32);
        f16x8 b1 = *(const f16x8*)(bp + 80);
        f16x8 A0 = __builtin_shufflevector(a0, a1, 0, 1, 2, 3, 4, 5, 6, 7);
        f16x8 A1 = __builtin_shufflevector(a2, a3, 0, 1, 2, 3, 4, 5, 6, 7);
        acc = __builtin_amdgcn_mfma_f32_32x32x16_f16(A0, b0, acc, 0, 0, 0);
        acc = __builtin_amdgcn_mfma_f32_32x32x16_f16(A1, b1, acc, 0, 0, 0);
        ap += 64; bp += 80;
    }
    {
        f16x4 a0 = *(const f16x4*)(ap), a1 = *(const f16x4*)(ap + 8);
        f16x8 b0 = *(const f16x8*)(bp + 32);
        f16x8 A0 = __builtin_shufflevector(a0, a1, 0, 1, 2, 3, 4, 5, 6, 7);
        acc = __builtin_amdgcn_mfma_f32_32x32x16_f16(A0, b0, acc, 0, 0, 0);
    }
    const float sc = kscale[d];
    u16* yo = y + (size_t)d * L + 1024 * TILE + 32 * r + 4 * h;
#pragma unroll
    for (int g = 0; g < 4; ++g) {
        u32x2 o;
        o.x = pkbf(acc[4 * g] * sc, acc[4 * g + 1] * sc);
        o.y = pkbf(acc[4 * g + 2] * sc, acc[4 * g + 3] * sc);
        *(u32x2*)(yo + 8 * g) = o;
    }
}

__global__ void __launch_bounds__(256) k2_conv(const u32x4* __restrict__ uT, const u32x4* __restrict__ Krev,
                                               const float* __restrict__ kscale, u16* __restrict__ y) {
    extern __shared__ __attribute__((aligned(16))) char smem[];
    const int lane = threadIdx.x & 63;
    const int wave = __builtin_amdgcn_readfirstlane(threadIdx.x >> 6);
    const int lb = (blockIdx.x & 7) * 30 + (blockIdx.x >> 3);
    const int unit = lb * 4 + wave;
    const int p = unit / 3, k = unit - 3 * p;
    char* lds = smem + wave * WAVE_LDS;
    if (k < 2) {
        conv_stage<1>(lds, uT, Krev, 2 * p + k, lane);
        __syncthreads();
        conv_run<1>(lds, y, kscale, 2 * p + k, lane);
    } else {
        conv_stage<0>(lds, uT, Krev, 2 * p, lane);
        conv_stage<0>(lds + 14080, uT, Krev, 2 * p + 1, lane);
        __syncthreads();
        conv_run<0>(lds, y, kscale, 2 * p, lane);
        conv_run<0>(lds + 14080, y, kscale, 2 * p + 1, lane);
    }
}

__global__ void __launch_bounds__(320) k3_ln2(const u16* __restrict__ x, const u16* __restrict__ y,
                                              const u16* __restrict__ dskip, const u16* __restrict__ wr,
                                              u16* __restrict__ out, unsigned* __restrict__ cand,
                                              unsigned* __restrict__ crow  ) {
    __shared__ float red[3][80];
    const int tid = threadIdx.x, lane = tid & 63, wave = tid >> 6;
    const int blk = (blockIdx.x & 7) * 32 + (blockIdx.x >> 3);
    const unsigned* xr = (const unsigned*)(x + (size_t)blk * 8 * D) + tid;
    const u32x4 ya = *(const u32x4*)(y + (size_t)(2 * tid) * L + blk * 8);
    const u32x4 yb = *(const u32x4*)(y + (size_t)(2 * tid + 1) * L + blk * 8);
    const unsigned ds = ((const unsigned*)dskip)[tid], w = ((const unsigned*)wr)[tid];
    float a[8], b[8];
#pragma unroll
    for (int t = 0; t < 8; ++t) { unsigned v = xr[t * (D / 2)]; a[t] = bflo(v); b[t] = bfhi(v); }
    float mb[8], sb[8];
    row_stats8(a, b, red[0], wave, lane, mb, sb);
    const float dsa = bflo(ds), dsb = bfhi(ds), wa = bflo(w), wb = bfhi(w);
#pragma unroll
    for (int t = 0; t < 8; ++t) {
        const float ua = rbf(rbf(a[t] - mb[t]) / sb[t]);
        const float ub = rbf(rbf(b[t] - mb[t]) / sb[t]);
        const float yav = (t & 1) ? bfhi(ya[t >> 1]) : bflo(ya[t >> 1]);
        const float ybv = (t & 1) ? bfhi(yb[t >> 1]) : bflo(yb[t >> 1]);
        a[t] = rbf(a[t] + rbf(yav + rbf(ua * dsa)));
        b[t] = rbf(b[t] + rbf(ybv + rbf(ub * dsb)));
    }
    row_stats8(a, b, red[1], wave, lane, mb, sb);
    float za[8], zb[8];
    unsigned* orow = (unsigned*)(out + (size_t)blk * 8 * D) + tid;
#pragma unroll
    for (int t = 0; t < 8; ++t) {
        za[t] = rbf(rbf(a[t] - mb[t]) / sb[t]);
        zb[t] = rbf(rbf(b[t] - mb[t]) / sb[t]);
        orow[t * (D / 2)] = pkbf(a[t] + za[t], b[t] + zb[t]);
        const float pr = wave_sum(fmaf(za[t], wa, zb[t] * wb));
        if (lane == 0) red[2][wave * 8 + t] = pr;
    }
    __syncthreads();
    unsigned best = 0;
#pragma unroll
    for (int t = 0; t < 8; ++t) {
        const float r = red[2][t] + red[2][8 + t] + red[2][16 + t] + red[2][24 + t] + red[2][32 + t];
        unsigned rb = f2bf(r);
        rb = (rb & 0x8000u) ? (~rb & 0xffffu) : (rb | 0x8000u);
        const unsigned key = (rb << 16) | (unsigned)(2047 - (blk * 8 + t));
        best = max(best, key);
    }
    const int bt = (2047 - (int)(best & 0xffffu)) & 7;
    float hs_a = a[0], hs_b = b[0], zs_a = za[0], zs_b = zb[0];
#pragma unroll
    for (int t = 1; t < 8; ++t) if (bt == t) { hs_a = a[t]; hs_b = b[t]; zs_a = za[t]; zs_b = zb[t]; }
    crow[blk * 640 + tid] = pkbf(zs_a, zs_b);
    crow[blk * 640 + 320 + tid] = pkbf(hs_a, hs_b);
    if (tid == 0) cand[blk] = best;
}

__global__ void __launch_bounds__(256) k4_gateup(const unsigned* __restrict__ cand, const unsigned* __restrict__ crow,
                                                 const u16* __restrict__ wg, const u16* __restrict__ wu,
                                                 u16* __restrict__ act) {
    __shared__ float xs[640];
    __shared__ float red[4][32];
    __shared__ unsigned bestw[4];
    const int tid = threadIdx.x, lane = tid & 63, wave = tid >> 6;
    const int f0 = blockIdx.x * 16;
    const int chunk = tid & 1, mat = (tid >> 1) & 1, rg = tid >> 2;
    const u16* wp = (mat ? wu : wg) + (size_t)rg * F + f0 + chunk * 8;
    u32x4 wv[10];
#pragma unroll
    for (int i = 0; i < 10; ++i) wv[i] = *(const u32x4*)(wp + (size_t)i * 64 * F);
    unsigned bk = wave_max_u32(cand[tid]);
    if (lane == 0) bestw[wave] = bk;
    __syncthreads();
    bk = max(max(bestw[0], bestw[1]), max(bestw[2], bestw[3]));
    const int blk = (2047 - (int)(bk & 0xffffu)) >> 3;
    for (int i = tid; i < 320; i += 256) {
        const unsigned v = crow[blk * 640 + i];
        xs[2 * i] = bflo(v); xs[2 * i + 1] = bfhi(v);
    }
    __syncthreads();
    float acc[8];
#pragma unroll
    for (int e = 0; e < 8; ++e) acc[e] = 0.f;
#pragma unroll
    for (int i = 0; i < 10; ++i) {
        const float xv = xs[rg + 64 * i];
#pragma unroll
        for (int e = 0; e < 4; ++e) {
            acc[2 * e] = fmaf(xv, bflo(wv[i][e]), acc[2 * e]);
            acc[2 * e + 1] = fmaf(xv, bfhi(wv[i][e]), acc[2 * e + 1]);
        }
    }
#pragma unroll
    for (int e = 0; e < 8; ++e) {
        float v = acc[e];
        v += __shfl_xor(v, 4); v += __shfl_xor(v, 8); v += __shfl_xor(v, 16); v += __shfl_xor(v, 32);
        acc[e] = v;
    }
    if (lane < 4) {
#pragma unroll
        for (int e = 0; e < 8; ++e) red[wave][lane * 8 + e] = acc[e];
    }
    __syncthreads();
    if (tid < 16) {
        const float g = rbf(red[0][tid] + red[1][tid] + red[2][tid] + red[3][tid]);
        const float u = rbf(red[0][16 + tid] + red[1][16 + tid] + red[2][16 + tid] + red[3][16 + tid]);
        const float s = rbf(g / (1.f + expf(-g)));
        act[f0 + tid] = (u16)f2bf(s * u);
    }
}

__global__ void __launch_bounds__(256) k5_down(const unsigned* __restrict__ cand, const unsigned* __restrict__ crow,
                                               const u16* __restrict__ act, const u16* __restrict__ wd,
                                               u16* __restrict__ out) {
    __shared__ float red[4][8];
    __shared__ unsigned bestw[4];
    const int tid = threadIdx.x, lane = tid & 63, wave = tid >> 6;
    const int j0 = blockIdx.x * 8;
    u32x4 wv[7]; float av[7];
#pragma unroll
    for (int i = 0; i < 7; ++i) {
        const int f = tid + 256 * i;
        if (f < F) { wv[i] = *(const u32x4*)(wd + (size_t)f * D + j0); av[i] = bf2f(act[f]); }
        else { wv[i] = (u32x4){0u, 0u, 0u, 0u}; av[i] = 0.f; }
    }
    unsigned bk = wave_max_u32(cand[tid]);
    if (lane == 0) bestw[wave] = bk;
    float acc[8];
#pragma unroll
    for (int e = 0; e < 8; ++e) acc[e] = 0.f;
#pragma unroll
    for (int i = 0; i < 7; ++i) {
#pragma unroll
        for (int e = 0; e < 4; ++e) {
            acc[2 * e] = fmaf(av[i], bflo(wv[i][e]), acc[2 * e]);
            acc[2 * e + 1] = fmaf(av[i], bfhi(wv[i][e]), acc[2 * e + 1]);
        }
    }
#pragma unroll
    for (int e = 0; e < 8; ++e) acc[e] = wave_sum(acc[e]);
    if (lane == 0) {
#pragma unroll
        for (int e = 0; e < 8; ++e) red[wave][e] = acc[e];
    }
    __syncthreads();
    if (tid < 8) {
        bk = max(max(bestw[0], bestw[1]), max(bestw[2], bestw[3]));
        const int row = 2047 - (int)(bk & 0xffffu);
        unsigned rb = bk >> 16;
        rb = (rb & 0x8000u) ? (rb & 0x7fffu) : (~rb & 0xffffu);
        const float rsel = rbf(1.f / (1.f + expf(-bf2f(rb))));
        const float yv = rbf(rbf(red[0][tid] + red[1][tid] + red[2][tid] + red[3][tid]) * rsel);
        const u16* cr = (const u16*)(crow + (row >> 3) * 640);
        const float z = bf2f(cr[j0 + tid]), h = bf2f(cr[640 + j0 + tid]);
        out[(size_t)row * D + j0 + tid] = (u16)f2bf(h + rbf(z + yv));
    }
}

extern "C" void kernel_launch(void* const* d_in, const int* in_sizes, int n_in, void* d_out, int out_size,
                              void* d_ws, size_t ws_size, hipStream_t stream) {
    const u16* x = (const u16*)d_in[0];
    const u16* logA = (const u16*)d_in[1];
    const u16* Aim = (const u16*)d_in[2];
    const u16* Cre = (const u16*)d_in[3];
    const u16* Cim = (const u16*)d_in[4];
    const u16* logdt = (const u16*)d_in[5];
    const u16* dskip = (const u16*)d_in[6];
    const u16* wr = (const u16*)d_in[8];
    const u16* wg = (const u16*)d_in[9];
    const u16* wu = (const u16*)d_in[10];
    const u16* wd = (const u16*)d_in[11];
    u16* out = (u16*)d_out;
    char* ws = (char*)d_ws;
    u32x4* uT = (u32x4*)ws;
    u32x4* Krev = (u32x4*)(ws + (4u << 20));
    u16* y = (u16*)(ws + (8u << 20));
    float* kscale = (float*)(ws + (12u << 20));
    unsigned* cand = (unsigned*)(ws + (12u << 20) + 4096);
    u16* act = (u16*)(ws + (12u << 20) + 8192);
    unsigned* crow = (unsigned*)(ws + (13u << 20));

    hipFuncSetAttribute((const void*)k2_conv, hipFuncAttributeMaxDynamicSharedMemorySize, 4 * WAVE_LDS);
    k1_ln_kgen<<<256 + 640, 320, 0, stream>>>(x, logA, Aim, Cre, Cim, logdt, uT, Krev, kscale);
    k2_conv<<<240, 256, 4 * WAVE_LDS, stream>>>(uT, Krev, kscale, y);
    k3_ln2<<<256, 320, 0, stream>>>(x, y, dskip, wr, out, cand, crow);
    k4_gateup<<<108, 256, 0, stream>>>(cand, crow, wg, wu, act);
    k5_down<<<80, 256, 0, stream>>>(cand, crow, act, wd, out);
}
```

```cpp
#include <hip/hip_runtime.h>
#include <hip/hip_bf16.h>
#include <stdint.h>

typedef _Float16 f16;
typedef f16 f16x2 __attribute__((ext_vector_type(2)));
typedef f16 f16x4 __attribute__((ext_vector_type(4)));
typedef f16 f16x8 __attribute__((ext_vector_type(8)));
typedef short s16x4 __attribute__((ext_vector_type(4)));
typedef __bf16 bf16x2 __attribute__((ext_vector_type(2)));
typedef __bf16 bf16x8 __attribute__((ext_vector_type(8)));
typedef float f32x2 __attribute__((ext_vector_type(2)));
typedef float f32x4 __attribute__((ext_vector_type(4)));
typedef float f32x16 __attribute__((ext_vector_type(16)));
typedef unsigned u32x2 __attribute__((ext_vector_type(2)));
typedef unsigned u32x4 __attribute__((ext_vector_type(4)));
typedef unsigned short u16;
typedef unsigned long long u64;
#define LAS __attribute__((address_space(3)))

constexpr int L = 2048, D = 640, NM = 128, F = 1728;

#define ST_ARG
#define STAMPC(i)
#define ST_PASS
#define STAMP_DECL
#define STAMP_END()
#define STAMP(i)

#define BAR() asm volatile("s_waitcnt lgkmcnt(0)\n\ts_barrier" ::: "memory")
#define BAR_NOWAIT() asm volatile("s_barrier" ::: "memory")
#define DRAIN() asm volatile("s_waitcnt vmcnt(0)" ::: "memory")
#define SB() __builtin_amdgcn_sched_barrier(0)

__device__ __forceinline__ float bf2f(unsigned x) { return __uint_as_float(x << 16); }
__device__ __forceinline__ f32x2 unpk(unsigned x) {
    f32x2 r; r.x = __uint_as_float(x << 16); r.y = __uint_as_float(x & 0xffff0000u); return r;
}
__device__ __forceinline__ float rbf(float x) { return (float)(__bf16)x; }
__device__ __forceinline__ unsigned f2bf(float x) { __bf16 b = (__bf16)x; return (unsigned)__builtin_bit_cast(u16, b); }
__device__ __forceinline__ unsigned pkbf(f32x2 v) {
    bf16x2 b = __builtin_convertvector(v, bf16x2); return __builtin_bit_cast(unsigned, b);
}
__device__ __forceinline__ f32x2 rbf2(f32x2 v) { return unpk(pkbf(v)); }
__device__ __forceinline__ unsigned pkh(float lo, float hi) {
    f32x2 v = {lo, hi}; f16x2 h = __builtin_convertvector(v, f16x2); return __builtin_bit_cast(unsigned, h);
}
__device__ __forceinline__ unsigned padd(unsigned a, unsigned b) {
    return __builtin_bit_cast(unsigned, __builtin_bit_cast(f16x2, a) + __builtin_bit_cast(f16x2, b));
}
__device__ __forceinline__ float dot2bf(unsigned a, unsigned b, float acc) {
    return __builtin_amdgcn_fdot2_f32_bf16(__builtin_bit_cast(bf16x2, a), __builtin_bit_cast(bf16x2, b), acc, false);
}
__device__ __forceinline__ f16x4 trread(const char* p) {
    return __builtin_bit_cast(f16x4, __builtin_amdgcn_ds_read_tr16_b64_v4i16((LAS s16x4*)p));
}
__device__ __forceinline__ f16x8 cat(f16x4 a, f16x4 b) { return __builtin_shufflevector(a, b, 0, 1, 2, 3, 4, 5, 6, 7); }

template <int CTRL, int RM = 0xf>
__device__ __forceinline__ float dppf(float v) {
    return __builtin_bit_cast(float, __builtin_amdgcn_update_dpp(0, __builtin_bit_cast(int, v), CTRL, RM, 0xf, false));
}
template <int CTRL, int RM = 0xf>
__device__ __forceinline__ unsigned dppu(unsigned v) {
    return (unsigned)__builtin_amdgcn_update_dpp(0, (int)v, CTRL, RM, 0xf, false);
}
__device__ __forceinline__ float row16_sum(float v) {
    v += dppf<0xB1>(v); v += dppf<0x4E>(v); v += dppf<0x141>(v); v += dppf<0x140>(v);
    return v;
}
__device__ __forceinline__ float wave_sum_u(float v) {
    v = row16_sum(v);
    v += dppf<0x142, 0xa>(v);
    v += dppf<0x143, 0xc>(v);
    return __builtin_bit_cast(float, __builtin_amdgcn_readlane(__builtin_bit_cast(int, v), 63));
}
__device__ __forceinline__ unsigned wave_max_u(unsigned v) {
    v = max(v, dppu<0xB1>(v)); v = max(v, dppu<0x4E>(v)); v = max(v, dppu<0x141>(v)); v = max(v, dppu<0x140>(v));
    v = max(v, dppu<0x142, 0xa>(v)); v = max(v, dppu<0x143, 0xc>(v));
    return (unsigned)__builtin_amdgcn_readlane((int)v, 63);
}
__device__ __forceinline__ f32x2 cmul(f32x2 a, f32x2 b) {
    f32x2 o;
    asm("v_pk_mul_f32 %0, %1, %2 op_sel_hi:[0,1]\n\t"
        "v_pk_fma_f32 %0, %1, %2, %0 op_sel:[1,1,0] op_sel_hi:[1,0,1] neg_lo:[0,1,0]"
        : "=&v"(o) : "v"(a), "v"(b));
    return o;
}
__device__ __forceinline__ float exp_acc(float x) {
    const float l2e = 1.44269504088896341f, l2e_lo = 1.92596299e-8f;
    const float t = x * l2e;
    float e = fmaf(x, l2e, -t);
    e = fmaf(x, l2e_lo, e);
    const float r = __builtin_amdgcn_exp2f(t);
    return fmaf(r * e, 0.693147180559945f, r);
}
__device__ __forceinline__ float sigmoid_f(float x) {
    const float e = __builtin_amdgcn_exp2f(fminf(-x * 1.44269504088896341f, 126.f));
    return __builtin_amdgcn_rcpf(1.f + e);
}
__device__ __forceinline__ f32x2 cexp_p(float zl, float wh, float wl, float p) {
    const float mag = __builtin_amdgcn_exp2f(zl * p);
    float t = __builtin_amdgcn_fractf(wh * p);
    t = fmaf(wl, p, t);
    f32x2 o = {__builtin_amdgcn_cosf(t), __builtin_amdgcn_sinf(t)};
    return o * mag;
}
__device__ __forceinline__ void ln_finish(float piv, float s1, float s2, float& mean_b, float& rstd) {
    const float dm = s1 * (1.f / D);
    const float var = fmaxf(fmaf(-dm, dm, s2 * (1.f / D)), 0.f);
    mean_b = rbf(piv + dm);
    rstd = __builtin_amdgcn_rcpf(rbf(__builtin_amdgcn_sqrtf(rbf(rbf(var) + 1e-5f))));
}
__device__ __forceinline__ void row_stats(const f32x2 (&v)[5], float& mb, float& rs) {
    const float piv = __builtin_bit_cast(float, __builtin_amdgcn_readfirstlane(__builtin_bit_cast(int, v[0].x)));
    f32x2 s1 = {0.f, 0.f}, s2 = {0.f, 0.f};
#pragma unroll
    for (int i = 0; i < 5; ++i) { f32x2 d = v[i] - piv; s1 += d; s2 = __builtin_elementwise_fma(d, d, s2); }
    const float t1 = wave_sum_u(s1.x + s1.y), t2 = wave_sum_u(s2.x + s2.y);
    ln_finish(piv, t1, t2, mb, rs);
}

constexpr unsigned WS_EPOCH = 0;
constexpr unsigned FSTR = 128;
constexpr unsigned WS_FLAGS = 4096;
constexpr unsigned WS_FLAGS2 = WS_FLAGS + 256 * FSTR;
constexpr unsigned WS_KEYS = WS_FLAGS2 + 256 * FSTR;
constexpr unsigned WS_REC = 131072;
constexpr unsigned WS_UT = 4u << 20;
constexpr unsigned WS_Y = 8u << 20;
constexpr unsigned WS_END = 12u << 20;
constexpr unsigned SENT = 0xffffffffu;
constexpr int SPIN_MAX = 20000;

constexpr int MSTR = 272, TABSZ = NM * MSTR;
constexpr int WSTR = 3200, WSLOT = 4 * WSTR;
constexpr int WG = 3 * WSLOT;
constexpr int KB = 3 * TABSZ, KCS = 1344, T2 = 4 * KCS, T3 = T2 + 79 * 16, KSLOT = T3 + 256;
constexpr int UB = KB + 3 * KSLOT, UOFF = 17 * 80, USLOT = 81 * 80;
constexpr int TRB = UB + 3 * USLOT, RSTR = 1344;
constexpr int MISC = TRB + 8 * RSTR;
constexpr int LDS_BYTES = MISC + 1024;
static_assert(WG + 40960 <= KB && LDS_BYTES <= 163840 && KSLOT % 16 == 0, "lds map");

typedef __amdgpu_buffer_rsrc_t rsrc_t;
__device__ __forceinline__ void st16(rsrc_t r, unsigned off, u32x4 v) { __builtin_amdgcn_raw_buffer_store_b128(v, r, off, 0, 16); }
__device__ __forceinline__ void st8(rsrc_t r, unsigned off, u32x2 v) { __builtin_amdgcn_raw_buffer_store_b64(v, r, off, 0, 16); }
__device__ __forceinline__ void st4(rsrc_t r, unsigned off, unsigned v) { __builtin_amdgcn_raw_buffer_store_b32(v, r, off, 0, 16); }
__device__ __forceinline__ u32x4 ld16(rsrc_t r, unsigned off) { return __builtin_amdgcn_raw_buffer_load_b128(r, off, 0, 16); }
__device__ __forceinline__ u32x2 ld8(rsrc_t r, unsigned off) { return __builtin_amdgcn_raw_buffer_load_b64(r, off, 0, 16); }
__device__ __forceinline__ unsigned ld4(rsrc_t r, unsigned off) { return __builtin_amdgcn_raw_buffer_load_b32(r, off, 0, 16); }
__device__ __forceinline__ bool pending(u32x4 v) { return max(max(v.x, v.y), max(v.z, v.w)) == SENT; }
__device__ __forceinline__ u32x4 poll16(rsrc_t r, unsigned off, u32x4 v) {
    for (int s = 0; s < SPIN_MAX; ++s) {
        const bool pend = pending(v);
        if (!__any(pend)) break;
        __builtin_amdgcn_s_sleep(2);
        asm volatile("" ::: "memory");
        if (pend) v = ld16(r, off);
    }
    return v;
}
template <typename BAD>
__device__ __forceinline__ void sweep4(rsrc_t r, unsigned base, int lane, unsigned (&f)[4], BAD bad) {
    const unsigned off = base + lane * FSTR;
#pragma unroll
    for (int k = 0; k < 4; ++k) f[k] = ld4(r, off + k * 64 * FSTR);
    for (int s = 0; s < SPIN_MAX; ++s) {
        if (!__any(bad(f[0]) || bad(f[1]) || bad(f[2]) || bad(f[3]))) break;
        __builtin_amdgcn_s_sleep(2);
        asm volatile("" ::: "memory");
#pragma unroll
        for (int k = 0; k < 4; ++k) if (bad(f[k])) f[k] = ld4(r, off + k * 64 * FSTR);
    }
}
__device__ __forceinline__ unsigned poll_keys(rsrc_t r, int lane) {
    unsigned f[4];
    sweep4(r, WS_KEYS, lane, f, [](unsigned v) { return v == SENT; });
    return wave_max_u(max(max(f[0], f[1]), max(f[2], f[3])));
}

#define MFMA32(a, b, c) __builtin_amdgcn_mfma_f32_32x32x16_f16(a, b, c, 0, 0, 0)

struct ConvPtr { const char *an, *bn, *a2, *b2, *a3, *b3; };
template <int TILE> struct Steps {
    static constexpr int NN = TILE ? 38 : 35, N2 = TILE ? 24 : 8, N3 = TILE ? 8 : 0, N = NN + N2 + N3;
};
template <int TILE, int S>
__device__ __forceinline__ void conv_ld(const ConvPtr& P, f16x8& A, f16x8& B) {
    using St = Steps<TILE>;
    if constexpr (S < St::NN) {
        constexpr int n = S - 1, nb = n > 33 ? n + 29 : n;
        constexpr int boff = (TILE ? 33 : 17) * 80 + ((nb & 1) ? -((nb + 1) / 2) * 80 + 32 : -(nb / 2) * 80);
        A = cat(trread(P.an + S * 32 + 8), trread(P.an + S * 32));
        B = *(const f16x8*)(P.bn + boff);
    } else if constexpr (S < St::NN + St::N2) {
        constexpr int s = S - St::NN;
        A = cat(trread(P.a2 + s * 128 + 32), trread(P.a2 + s * 128));
        B = *(const f16x8*)(P.b2 + (St::N2 - 1 - s) * 32);
    } else {
        constexpr int s = S - St::NN - St::N2;
        A = cat(trread(P.a3 + s * 256), trread(P.a3 + s * 256 + 64));
        B = cat(*(const f16x4*)(P.b3 + (7 - s) * 32 + 8), *(const f16x4*)(P.b3 + (7 - s) * 32));
    }
}
constexpr int PF = 4;
template <int TILE, int S>
__device__ __forceinline__ void conv_steps(const ConvPtr& P, f16x8 (&A)[PF], f16x8 (&B)[PF], f32x16& acc,
                                           volatile LAS unsigned* cnt, unsigned& seen ST_ARG) {
    using St = Steps<TILE>;
    if constexpr (S < St::N) {
        acc = MFMA32(A[S % PF], B[S % PF], acc);
        if constexpr (S + PF + 4 == St::NN) seen = *cnt;
        if constexpr (S + PF == St::NN) {
            SB();
            STAMP(12);
            for (int s = 0; s < SPIN_MAX && __builtin_amdgcn_readfirstlane(seen) < 8u; ++s) seen = *cnt;
            asm volatile("" ::: "memory");
            STAMP(1);
        }
        if constexpr (S + PF < St::N) conv_ld<TILE, S + PF>(P, A[S % PF], B[S % PF]);
        SB();
        conv_steps<TILE, S + 1>(P, A, B, acc, cnt, seen ST_PASS);
    }
}
template <int TILE>
__device__ __forceinline__ void conv_tile(char* smem, int slot, int ch, rsrc_t wsr, int lane ST_ARG) {
    const int c = lane & 31, h = lane >> 5, q = (lane >> 2) & 3;
    const int G = ((lane >> 4) & 1) * 4 + (lane & 3);
    const int tr = 16 * (G & 1) + 4 * (G >> 1);
    const char* wsl = smem + slot * WSLOT;
    const char* ks = smem + KB + slot * KSLOT;
    ConvPtr P;
    P.an = ks + q * KCS + 2 * (tr - 8 * h + 8);
    P.bn = smem + UB + slot * USLOT + UOFF + (32 * TILE + c - (TILE ? 33 : 17)) * 80 + 16 * h;
    P.a2 = wsl + (q & 1) * WSTR + 2 * (36 + 32 * h + tr - 4 * (q + 1) - 16);
    P.b2 = ks + T2 + (31 + 32 * TILE + c - 16 - h - 2 * (Steps<TILE>::N2 - 1)) * 16;
    P.a3 = wsl + (2 + (q & 1)) * WSTR + 2 * (36 + 64 * h + tr - 8 * (q + 1));
    P.b3 = ks + T3 + (c - 2 * h - 29) * 8;
    const float sc = *(const float*)(smem + MISC + 64 + slot * 4);
    f16x8 A[PF], B[PF];
    conv_ld<TILE, 0>(P, A[0], B[0]); conv_ld<TILE, 1>(P, A[1], B[1]);
    conv_ld<TILE, 2>(P, A[2], B[2]); conv_ld<TILE, 3>(P, A[3], B[3]);
    f32x16 acc;
#pragma unroll
    for (int i = 0; i < 16; ++i) acc[i] = 0.f;
    SB();
    unsigned seen = 0;
    conv_steps<TILE, 0>(P, A, B, acc, (volatile LAS unsigned*)(smem + MISC + 48), seen ST_PASS);
#pragma unroll
    for (int g = 0; g < 2; ++g) {
        u32x4 o;
#pragma unroll
        for (int e = 0; e < 4; ++e) o[e] = pkbf((f32x2){acc[8 * g + 2 * e], acc[8 * g + 2 * e + 1]} * sc);
        st16(wsr, WS_Y + (ch * 256 + TILE * 128 + g * 64 + lane) * 16, o);
    }
}

template <bool FULL>
__device__ __forceinline__ void conv_windows(char* smem, int slot, int i, int lane) {
    const char* op = smem + UB + slot * USLOT + UOFF + (lane >> 2) * 80 + (lane & 3) * 16 + i * 1280;
    const u32x4 own = *(const u32x4*)op;
    const u32x4 pv = *(const u32x4*)(op - ((lane & 3) ? 16 : 32));
    unsigned w[8] = {pv.x, pv.y, pv.z, pv.w, own.x, own.y, own.z, own.w};
    unsigned a[7];
#pragma unroll
    for (int k = 0; k < 7; ++k) a[k] = __builtin_amdgcn_alignbit(w[k + 1], w[k], 16);
    u32x4 w3, w5, w7, w9;
#pragma unroll
    for (int k = 0; k < 4; ++k) {
        w3[k] = padd(w[2 + k], padd(a[1 + k], a[2 + k]));
        w5[k] = padd(w3[k], padd(w[1 + k], w[3 + k]));
        if (FULL) {
            w7[k] = padd(w5[k], padd(a[k], a[3 + k]));
            w9[k] = padd(w7[k], padd(w[k], w[4 + k]));
        }
    }
    char* wp = smem + slot * WSLOT + 64 + lane * 16 + i * 1024;
    *(u32x4*)(wp) = w3;
    *(u32x4*)(wp + WSTR) = w5;
    if (FULL) {
        *(u32x4*)(wp + 2 * WSTR) = w7;
        *(u32x4*)(wp + 3 * WSTR) = w9;
    }
}

__global__ void __launch_bounds__(1024, 4)
fused_kernel(const u16* __restrict__ x, const u16* __restrict__ logA, const u16* __restrict__ Aim,
             const u16* __restrict__ Cre, const u16* __restrict__ Cim, const u16* __restrict__ logdt,
             const u16* __restrict__ dskip, const u16* __restrict__ wr, const u16* __restrict__ wg,
             const u16* __restrict__ wu, const u16* __restrict__ wd, u16* out, char* ws) {
    extern __shared__ __attribute__((aligned(16))) char smem[];
    const int tid = threadIdx.x, lane = tid & 63;
    const int wave = __builtin_amdgcn_readfirstlane(tid >> 6);
    const int p = (blockIdx.x & 7) * 32 + (blockIdx.x >> 3);
    const int isB = p & 1;
    const int d0 = 5 * (p >> 1) + 2 * isB;
    const int fs = (p >> 5) * 14 + (p & 31);
    const bool ffn = (p & 31) < 14 && fs < 108;
    asm volatile("" :: "s"(wg), "s"(wu), "s"(wd), "s"(out), "s"(ws), "s"(dskip), "s"(wr));
    const rsrc_t wsr = __builtin_amdgcn_make_buffer_rsrc(ws, 0, WS_END, 0x00020000);
    STAMP_DECL; STAMP(0);

    if (wave < 12) {
        auto ld5 = [&](const u16* q_, unsigned (&o)[5]) {
            u32x2 a = *(const u32x2*)(q_ + lane * 4), b = *(const u32x2*)(q_ + 256 + lane * 4);
            o[0] = a.x; o[1] = a.y; o[2] = b.x; o[3] = b.y; o[4] = *(const unsigned*)(q_ + 512 + lane * 2);
        };
        const int row = p * 8 + (wave & 7);
        unsigned xr[5], er[5], wv[5];
        if (wave < 8) ld5(x + (size_t)row * D, xr);
        const int slot = wave >> 2, sub = wave & 3;
        const int dch = isB ? d0 + (slot == 2 ? 0 : slot + 1) : d0 + slot;
        const int n = (sub & 1) * 64 + lane;
        unsigned la_ = logA[dch * NM + n], ai_ = Aim[dch * NM + n], ldt_ = logdt[dch];
        unsigned c0 = 0, c1 = 0, cr_ = 0, ci_ = 0;
        if (sub >= 2) {
            c0 = ((const unsigned*)(Cre + dch * NM))[lane];
            c1 = ((const unsigned*)(Cim + dch * NM))[lane];
            cr_ = Cre[dch * NM + n]; ci_ = Cim[dch * NM + n];
        }
        if (wave < 8) {
            ld5(dskip, er);
            ld5(wr, wv);
            SB();
            f32x2 v[5];
#pragma unroll
            for (int i = 0; i < 5; ++i) v[i] = unpk(xr[i]);
            STAMP(1);
            float mb, rs;
            row_stats(v, mb, rs);
            unsigned uh[5];
#pragma unroll
            for (int i = 0; i < 5; ++i) {
                v[i] = rbf2(rbf2(v[i] - mb) * rs);
                uh[i] = pkh(v[i].x, v[i].y);
            }
            char* tp = smem + TRB + wave * RSTR;
            *(u32x2*)(tp + lane * 8) = (u32x2){uh[0], uh[1]};
            *(u32x2*)(tp + 512 + lane * 8) = (u32x2){uh[2], uh[3]};
            *(unsigned*)(tp + 1024 + lane * 4) = uh[4];
            SB();
#pragma unroll
            for (int i = 0; i < 5; ++i) er[i] = pkbf(v[i] * unpk(er[i]));
#pragma unroll
            for (int i = 0; i < 5; ++i) asm volatile("" : "+v"(wv[i]));
        }
        asm volatile("" : "+v"(la_), "+v"(ai_), "+v"(ldt_), "+v"(c0), "+v"(c1), "+v"(cr_), "+v"(ci_));
        BAR();
        STAMP(2);
        if (wave < 10) {
            const char* tp = smem + TRB + ((lane >> 2) & 3) * RSTR + ((lane >> 4) * 16 + (lane & 3) * 4) * 2 + wave * 128;
            f16x8 o = cat(trread(tp), trread(tp + 4 * RSTR));
            st16(wsr, WS_UT + (p * D + tid) * 16, __builtin_bit_cast(u32x4, o));
        }
        STAMP(3);
        {
            const float dt = rbf(exp_acc(bf2f(ldt_)));
            const float zr = -rbf(exp_acc(bf2f(la_))) * dt;
            const float zi = bf2f(ai_) * dt;
            const float zl = zr * 1.44269504088896341f;
            const float wh = zi * 0.15915494309189535f;
            const float wl = fmaf(zi, 6.4206383e-09f, fmaf(zi, 0.15915494309189535f, -wh));
            const f32x2 e128 = cexp_p(zl, wh, wl, 128.f);
            const f32x2 e256 = cmul(e128, e128), e384 = cmul(e128, e256), e512 = cmul(e256, e256);
            char* tb = smem + slot * TABSZ + n * MSTR;
            auto put8 = [&](int grp, const f32x2 (&e)[8]) {
                u32x4 re = {pkh(e[0].x, e[1].x), pkh(e[2].x, e[3].x), pkh(e[4].x, e[5].x), pkh(e[6].x, e[7].x)};
                u32x4 im = {pkh(e[0].y, e[1].y), pkh(e[2].y, e[3].y), pkh(e[4].y, e[5].y), pkh(e[6].y, e[7].y)};
                *(u32x4*)(tb + grp * 16) = re;
                *(u32x4*)(tb + 128 + grp * 16) = im;
            };
            if (sub < 2) {
                f32x2 e[8], g[8];
                e[0] = (f32x2){1.f, 0.f};
                e[1] = cexp_p(zl, wh, wl, 1.f);
                e[2] = cmul(e[1], e[1]); e[4] = cmul(e[2], e[2]);
                const f32x2 e8 = cmul(e[4], e[4]);
                e[3] = cmul(e[1], e[2]); e[5] = cmul(e[1], e[4]); e[6] = cmul(e[2], e[4]); e[7] = cmul(e[3], e[4]);
                put8(0, e);
                g[0] = e128;
#pragma unroll
                for (int r = 1; r < 8; ++r) g[r] = cmul(e[r], e128);
                put8(1, g);
                g[0] = cmul(e[2], e256); g[1] = cmul(e[4], e256); g[2] = cmul(e[6], e256); g[3] = cmul(e8, e256);
                g[4] = cmul(e[2], e384); g[5] = cmul(e[4], e384); g[6] = cmul(e[6], e384); g[7] = cmul(e8, e384);
                put8(2, g);
                g[0] = cmul(e512, e[4]); g[1] = cmul(e512, e8);
                g[2] = cmul(g[0], e128); g[3] = cmul(g[1], e128);
                g[4] = cmul(g[0], e256); g[5] = cmul(g[1], e256);
                g[6] = cmul(g[0], e384); g[7] = cmul(g[1], e384);
                put8(3, g);
            } else {
                c0 &= 0x7fff7fffu; c1 &= 0x7fff7fffu;
                const unsigned mx = wave_max_u(max(max(c0 >> 16, c0 & 0xffffu), max(c1 >> 16, c1 & 0xffffu)));
                const int ex = min(max((int)(mx >> 7), 8), 246);
                const float up = __uint_as_float((unsigned)(260 - ex) << 23);
                if (lane == 0) *(float*)(smem + MISC + 64 + slot * 4) = 2.f * dt * __uint_as_float((unsigned)(ex - 6) << 23);
                const f32x2 e8 = cexp_p(zl, wh, wl, 8.f);
                const f32x2 c8 = {e8.x, -e8.y};
                const f32x2 c16 = cmul(c8, c8), c32 = cmul(c16, c16), c64 = cmul(c32, c32);
                const f32x2 e1024 = cmul(e512, e512);
                f32x2 b[8], g[8];
                g[0] = cmul(e1024, e8); g[1] = cmul(g[0], e128); g[2] = cmul(g[0], e256); g[3] = cmul(g[0], e384);
#pragma unroll
                for (int s = 0; s < 4; ++s) g[4 + s] = cmul(g[s], e512);
                put8(4, g);
                *(u32x2*)(tb + 80) = (u32x2){pkh(e256.x, e512.x), pkh(e1024.x, 0.f)};
                *(u32x2*)(tb + 128 + 80) = (u32x2){pkh(e256.y, e512.y), pkh(e1024.y, 0.f)};
                b[0] = (f32x2){bf2f(cr_) * up, -bf2f(ci_) * up};
                b[1] = cmul(b[0], c8); b[2] = cmul(b[0], c16); b[3] = cmul(b[1], c16);
#pragma unroll
                for (int j = 0; j < 4; ++j) b[4 + j] = cmul(b[j], c32);
                put8(6, b);
#pragma unroll
                for (int j = 0; j < 8; ++j) g[j] = cmul(b[j], c64);
                put8(7, g);
            }
        }
        const int ug = tid / 3, uc = tid - 3 * ug;
        const unsigned uoff = WS_UT + (ug * D + d0 + uc) * 16;
        STAMP(4);
        BAR();
        STAMP(5);
        u32x4 uv = ld16(wsr, uoff);
        SB();
        if (sub < 3) {
            char* ks = smem + KB + slot * KSLOT;
            const int qq = lane >> 4, q = (lane >> 2) & 3, pp = lane & 3, j = lane & 15;
            const char* ap = smem + slot * TABSZ + (4 * qq + 2 * (q >> 1)) * MSTR + (q & 1) * 128 + pp * 8;
            const char* bp = ap + (sub == 2 ? 64 : 96);
            ap += sub == 2 ? 96 : sub * 32;
            f16x8 av[8], bv[8];
#pragma unroll
            for (int k = 0; k < 8; ++k) {
                av[k] = cat(trread(ap + k * 16 * MSTR), trread(ap + k * 16 * MSTR + MSTR));
                bv[k] = cat(trread(bp + k * 16 * MSTR), trread(bp + k * 16 * MSTR + MSTR));
            }
            SB();
            f32x4 acc = {0.f, 0.f, 0.f, 0.f}, acc1 = {0.f, 0.f, 0.f, 0.f};
#pragma unroll
            for (int k = 0; k < 4; ++k) {
                acc = __builtin_amdgcn_mfma_f32_16x16x32_f16(av[2 * k], bv[2 * k], acc, 0, 0, 0);
                acc1 = __builtin_amdgcn_mfma_f32_16x16x32_f16(av[2 * k + 1], bv[2 * k + 1], acc1, 0, 0, 0);
            }
            acc += acc1;
            const unsigned p01 = pkh(acc[0], acc[1]), p23 = pkh(acc[2], acc[3]), p12 = pkh(acc[1], acc[2]);
            if (sub == 0) {
                char* o = ks + 2 * (28 + 4 * (qq & 1) + 128 * (qq >> 1) + 8 * j);
                *(u32x2*)o = (u32x2){p01, p23};
                *(unsigned*)(o + 2 * KCS + 4) = p01; *(unsigned*)(o + 2 * KCS + 8) = p23;
                *(u16*)(o + KCS + 2) = (u16)p01; *(unsigned*)(o + KCS + 4) = p12; *(u16*)(o + KCS + 8) = (u16)(p23 >> 16);
                *(u16*)(o + 3 * KCS + 6) = (u16)p01; *(unsigned*)(o + 3 * KCS + 8) = p12; *(u16*)(o + 3 * KCS + 12) = (u16)(p23 >> 16);
            } else if (sub == 1) {
                if (qq < 2) {
                    const unsigned p11 = pkh(acc[1], acc[1]), p33 = pkh(acc[3], acc[3]);
                    char* o = ks + 2 * (28 + 258 + 128 * qq + 8 * j);
                    unsigned* o0 = (unsigned*)o;
                    o0[0] = p01; o0[1] = p11; o0[2] = p23; o0[3] = p33;
                    *(u32x4*)(o + 2 * KCS + 4) = (u32x4){p01, p11, p23, p33};
                    *(u16*)(o + KCS + 2) = (u16)p01; *(u32x2*)(o + KCS + 4) = (u32x2){p11, p12};
                    *(unsigned*)(o + KCS + 12) = p33; *(u16*)(o + KCS + 16) = (u16)p33;
                    *(u16*)(o + 3 * KCS + 6) = (u16)p01; *(unsigned*)(o + 3 * KCS + 8) = p11;
                    *(u32x2*)(o + 3 * KCS + 12) = (u32x2){p12, p33}; *(u16*)(o + 3 * KCS + 20) = (u16)p33;
                } else {
                    unsigned* t2 = (unsigned*)(ks + T2 + 31 * 16) + 32 * (qq - 2) + j;
                    t2[0] = p01; t2[16] = p23;
                }
            } else {
                if (j < 8) *(u32x2*)(ks + T3 + (16 * j + 4 * qq) * 2) = (u32x2){p01, p23};
                const unsigned s256 = __builtin_amdgcn_readlane((int)p01, 8), s512 = __builtin_amdgcn_readlane((int)p01, 9),
                               s1024 = __builtin_amdgcn_readlane((int)p01, 10);
                const int w = lane >> 2, cq = lane & 3;
                const int lg = w < 2 ? 256 + w : (w == 2 ? 514 : 560 + w);
                const unsigned sv = w < 2 ? s256 : (w == 2 ? s512 : s1024);
                if (w < 5) *(u16*)(ks + cq * KCS + 2 * (lg + cq + 28)) = (u16)sv;
            }
        }
        STAMP(6);
        uv = poll16(wsr, uoff, uv);
        STAMP(7);
        {
            const int us = isB ? (uc == 0 ? 2 : uc - 1) : uc;
            *(u32x4*)(smem + UB + us * USLOT + UOFF + (ug >> 2) * 80 + (ug & 3) * 16) = uv;
        }
        BAR();
        STAMP(8);
        unsigned wdp[16];
        if (wave < 8) {
            if (wave < 6) conv_windows<true>(smem, wave >> 1, wave & 1, lane);
            else {
                conv_windows<false>(smem, wave - 6, 2, lane);
                if (wave == 6) conv_windows<false>(smem, 2, 2, lane);
                else if (lane < 48) *(u32x4*)(smem + (lane >> 2) * WSTR + (lane & 3) * 16) = (u32x4){0u, 0u, 0u, 0u};
            }
            asm volatile("s_waitcnt lgkmcnt(0)" ::: "memory");
            if (lane == 0) __hip_atomic_fetch_add((LAS unsigned*)(smem + MISC + 48), 1u, __ATOMIC_RELAXED, __HIP_MEMORY_SCOPE_WORKGROUP);
            STAMP(9);
            if (ffn && wave < 5) {
                const unsigned* wp = (const unsigned*)(wd + (size_t)fs * 16 * D) + tid;
#pragma unroll
                for (int i = 0; i < 16; ++i) wdp[i] = wp[i * (D / 2)];
            }
        } else {
            STAMPC(2);
            const int mw = wave - 8;
            auto chof = [&](int s) { return isB ? d0 + (s == 2 ? 0 : s + 1) : d0 + s; };
            if (mw < 2 || (mw == 2 && !isB)) {
                conv_tile<1>(smem, mw, chof(mw), wsr, lane ST_PASS);
            } else {
                const int s0 = mw == 2 ? 2 : 0, ns = mw == 2 ? 1 : 2;
#pragma unroll 1
                for (int it = 0; it < ns; ++it) conv_tile<0>(smem, s0 + it, chof(s0 + it), wsr, lane ST_PASS);
            }
            STAMPC(3);
            STAMP(9);
            DRAIN();
        }
        BAR();
        STAMP(10);
        if (wave < 10) {
            const unsigned yoff = WS_Y + (tid * 256 + (p >> 7) * 128 + (p & 1) * 64 + ((p >> 1) & 1) * 32 + ((p >> 2) & 31)) * 16;
            const u32x4 yv = poll16(wsr, yoff, ld16(wsr, yoff));
            u16* yp = (u16*)(smem + TRB) + tid;
#pragma unroll
            for (int i = 0; i < 4; ++i) {
                yp[(2 * i) * D] = (u16)yv[i];
                yp[(2 * i + 1) * D] = (u16)(yv[i] >> 16);
            }
        }
        STAMP(11);
        BAR();
        if (wave < 8) {
            unsigned yr[5], hp[5], zp[5], op[5];
            ld5((const u16*)(smem + TRB) + wave * D, yr);
            f32x2 hv[5];
#pragma unroll
            for (int i = 0; i < 5; ++i) {
                hp[i] = pkbf(unpk(xr[i]) + rbf2(unpk(yr[i]) + unpk(er[i])));
                hv[i] = unpk(hp[i]);
            }
            float mb, rs;
            row_stats(hv, mb, rs);
            float ra = 0.f;
#pragma unroll
            for (int i = 0; i < 5; ++i) {
                zp[i] = pkbf(rbf2(hv[i] - mb) * rs);
                ra = dot2bf(zp[i], wv[i], ra);
            }
            const float r = wave_sum_u(ra);
            unsigned rb = f2bf(r);
            rb = (rb & 0x8000u) ? (~rb & 0xffffu) : (rb | 0x8000u);
            const unsigned key = (rb << 16) | (unsigned)(2047 - row);
            if (lane == 0) *(unsigned*)(smem + MISC + wave * 4) = key;
            BAR();
            STAMP(12);
            const u32x4 k0 = *(const u32x4*)(smem + MISC), k1 = *(const u32x4*)(smem + MISC + 16);
#pragma unroll
            for (int i = 0; i < 5; ++i) op[i] = pkbf(hv[i] + unpk(zp[i]));
            const unsigned best = max(max(max(k0.x, k0.y), max(k0.z, k0.w)), max(max(k1.x, k1.y), max(k1.z, k1.w)));
            if (__builtin_amdgcn_readfirstlane(best) == key) {
                const unsigned rec = WS_REC + p * 5120;
                if (lane == 0) st4(wsr, WS_KEYS + p * FSTR, key);
                st8(wsr, rec + lane * 8, (u32x2){zp[0], zp[1]});
                st8(wsr, rec + 512 + lane * 8, (u32x2){zp[2], zp[3]});
                st4(wsr, rec + 1024 + lane * 4, zp[4]);
                const rsrc_t orr = __builtin_amdgcn_make_buffer_rsrc(out + (size_t)row * D, 0, D * 2, 0x00020000);
                st8(orr, lane * 8, (u32x2){op[0], op[1]});
                st8(orr, 512 + lane * 8, (u32x2){op[2], op[3]});
                st4(orr, 1024 + lane * 4, op[4]);
                DRAIN();
                st8(wsr, rec + 1280 + lane * 8, (u32x2){hp[0], hp[1]});
                st8(wsr, rec + 1280 + 512 + lane * 8, (u32x2){hp[2], hp[3]});
                st4(wsr, rec + 1280 + 1024 + lane * 4, hp[4]);
            } else {
                u16* q_ = out + (size_t)row * D;
                *(u32x2*)(q_ + lane * 4) = (u32x2){op[0], op[1]};
                *(u32x2*)(q_ + 256 + lane * 4) = (u32x2){op[2], op[3]};
                *(unsigned*)(q_ + 512 + lane * 2) = op[4];
            }
        } else {
            BAR();
        }
        if (ffn) {
            f16x8 bw[4];
            const int mat = wave >= 5, kg = wave - 5 * mat;
            if (wave < 10) {
                const char* bp = smem + WG + mat * 20480 + (kg * 128 + (lane >> 4) * 8 + ((lane >> 2) & 3)) * 32 + (lane & 3) * 8;
#pragma unroll
                for (int k = 0; k < 4; ++k) bw[k] = cat(trread(bp + k * 1024), trread(bp + k * 1024 + 128));
            }
            unsigned wq[16];
            if (wave < 5) {
#pragma unroll
                for (int i = 0; i < 8; ++i) {
                    wq[2 * i] = __builtin_amdgcn_perm(wdp[2 * i + 1], wdp[2 * i], 0x05040100u);
                    wq[2 * i + 1] = __builtin_amdgcn_perm(wdp[2 * i + 1], wdp[2 * i], 0x07060302u);
                }
            }
            BAR();
            STAMP(13);
            if (wave < 10) {
                const unsigned bk = *(const unsigned*)(smem + MISC + 32);
                const unsigned xo = WS_REC + ((2047u - (bk & 2047u)) >> 3) * 5120 + (kg * 128 + (lane >> 4) * 8) * 2;
                u32x4 xa[4];
#pragma unroll
                for (int k = 0; k < 4; ++k) xa[k] = ld16(wsr, xo + k * 64);
#pragma unroll
                for (int k = 0; k < 4; ++k) xa[k] = poll16(wsr, xo + k * 64, xa[k]);
                f32x4 acc = {0.f, 0.f, 0.f, 0.f};
#pragma unroll
                for (int k = 0; k < 4; ++k)
                    acc = __builtin_amdgcn_mfma_f32_16x16x32_bf16(__builtin_bit_cast(bf16x8, xa[k]), __builtin_bit_cast(bf16x8, bw[k]), acc, 0, 0, 0);
                if (lane < 16) *(float*)(smem + MISC + 128 + (wave * 16 + lane) * 4) = acc[0];
            }
            BAR();
            STAMP(14);
            if (wave < 5) {
                const float* pr = (const float*)(smem + MISC + 128) + (lane & 15);
                const float g = rbf(pr[0] + pr[16] + pr[32] + pr[48] + pr[64]);
                const float u = rbf(pr[80] + pr[96] + pr[112] + pr[128] + pr[144]);
                const unsigned a = f2bf(rbf(g * sigmoid_f(g)) * u);
                const unsigned ap = a | (dppu<0xB1>(a) << 16);
                float y0 = 0.f, y1 = 0.f;
#pragma unroll
                for (int i = 0; i < 8; ++i) {
                    const unsigned s = (unsigned)__builtin_amdgcn_readlane((int)ap, 2 * i);
                    y0 = dot2bf(wq[2 * i], s, y0);
                    y1 = dot2bf(wq[2 * i + 1], s, y1);
                }
                st8(wsr, WS_REC + p * 5120 + 2560 + tid * 8, (u32x2){__float_as_uint(y0), __float_as_uint(y1)});
            }
            STAMP(15);
        }
    } else {
        const int xi = tid - 768;
        const u32x4 s4 = {SENT, SENT, SENT, SENT};
        unsigned tag = 0;
        if (wave == 15) {
            const unsigned ep = ld4(wsr, WS_EPOCH);
#pragma unroll
            for (int i = 0; i < 10; ++i) st16(wsr, WS_UT + (p * D + i * 64 + lane) * 16, s4);
            DRAIN();
            tag = __builtin_amdgcn_readfirstlane(ep) + 1u;
            if (lane == 0) st4(wsr, WS_FLAGS + p * FSTR, tag);
            STAMP(1);
        } else {
            const u32x4 z = {0u, 0u, 0u, 0u};
            char* kz = smem + KB + (wave - 12) * KSLOT + lane * 16;
#pragma unroll
            for (int i = 0; i < 6; ++i) *(u32x4*)(kz + i * 1024) = z;
            if (lane < (KSLOT - 6144) / 16) *(u32x4*)(kz + 6144) = z;
            char* uz = smem + UB + (wave - 12) * USLOT + lane * 16;
            *(u32x4*)uz = z;
            if (lane < 21) *(u32x4*)(uz + 1024) = z;
            if (xi == 0) *(unsigned*)(smem + MISC + 48) = 0u;
        }
        BAR();
        if (wave == 15) {
            unsigned f[4];
            sweep4(wsr, WS_FLAGS, lane, f, [&](unsigned v) { return v != tag; });
            if (p == 0 && lane == 0) st4(wsr, WS_EPOCH, tag);
            STAMP(4);
        } else {
            if (wave < 14) {
#pragma unroll
                for (int i = 0; i < 5; ++i) {
                    const int ii = (wave - 12) * 5 + i;
                    const int k = isB ? (ii < 2 ? ii : ii + 2) : (ii < 8 ? ii : ii + 2);
                    st16(wsr, WS_Y + (d0 * 256 + k * 64 + lane) * 16, s4);
                }
            } else {
#pragma unroll
                for (int i = 0; i < 5; ++i) st16(wsr, WS_REC + p * 5120 + (i * 64 + lane) * 16, s4);
                if (lane == 0) st4(wsr, WS_KEYS + p * FSTR, SENT);
            }
        }
        BAR();
        DRAIN();
        STAMP(6);
        BAR();
        if (wave == 15 && lane == 0) st4(wsr, WS_FLAGS2 + p * FSTR, tag);
        u32x4 wv[10];
        if (ffn) {
#pragma unroll
            for (int i = 0; i < 10; ++i) {
                const u16* wsrc = i < 5 ? wg : wu;
                wv[i] = *(const u32x4*)(wsrc + (size_t)((i % 5) * 128 + (xi >> 1)) * F + fs * 16 + (xi & 1) * 8);
            }
        }
        STAMP(8);
        if (wave == 15) {
            __builtin_amdgcn_s_sleep(16);
            unsigned f[4];
            sweep4(wsr, WS_FLAGS2, lane, f, [&](unsigned v) { return v != tag; });
            STAMP(9);
        }
        BAR();
        if (ffn) {
#pragma unroll
            for (int i = 0; i < 10; ++i) *(u32x4*)(smem + WG + (i * 256 + xi) * 16) = wv[i];
        }
        BAR();
        BAR();
        STAMP(12);
        if (ffn) {
            if (wave == 15) {
                const unsigned bk = poll_keys(wsr, lane);
                if (lane == 0) *(unsigned*)(smem + MISC + 32) = bk;
            }
            BAR();
            BAR();
        } else if ((p & 31) >= 16 && (p & 31) < 26 && wave < 14) {
            const int cq = 2 * ((p >> 5) * 10 + (p & 31) - 16) + (wave - 12);
            __builtin_amdgcn_s_sleep(40);
            const unsigned bk = poll_keys(wsr, lane);
            const int row = 2047 - (int)(bk & 2047u);
            const unsigned zo = WS_REC + (row >> 3) * 5120 + cq * 8 + (lane & 1) * 1280;
            u32x2 zh = ld8(wsr, zo);
            const int f0 = min(lane, 53), f1 = f0 + 54;
            const unsigned po0 = WS_REC + ((f0 / 14) * 32 + f0 % 14) * 5120 + 2560 + cq * 16;
            const unsigned po1 = WS_REC + ((f1 / 14) * 32 + f1 % 14) * 5120 + 2560 + cq * 16;
            for (int s = 0; s < SPIN_MAX; ++s) {
                const bool pend = max(zh.x, zh.y) == SENT;
                if (!__any(pend)) break;
                __builtin_amdgcn_s_sleep(2);
                asm volatile("" ::: "memory");
                if (pend) zh = ld8(wsr, zo);
            }
            unsigned rb = bk >> 16;
            rb = (rb & 0x8000u) ? (rb & 0x7fffu) : (~rb & 0xffffu);
            const float rsel = rbf(sigmoid_f(bf2f(rb)));
            STAMP(13);
            u32x4 v0 = ld16(wsr, po0), v1 = ld16(wsr, po1);
            v0 = poll16(wsr, po0, v0);
            v1 = poll16(wsr, po1, v1);
            STAMP(14);
            f32x4 sv = __builtin_bit_cast(f32x4, v0) + __builtin_bit_cast(f32x4, v1);
            if (lane >= 54) sv = (f32x4){0.f, 0.f, 0.f, 0.f};
            float sm[4];
#pragma unroll
            for (int e = 0; e < 4; ++e) sm[e] = wave_sum_u(sv[e]);
            const unsigned z01 = __builtin_amdgcn_readlane((int)zh.x, 0), z23 = __builtin_amdgcn_readlane((int)zh.y, 0);
            const unsigned h01 = __builtin_amdgcn_readlane((int)zh.x, 1), h23 = __builtin_amdgcn_readlane((int)zh.y, 1);
            const f32x2 ya = rbf2(rbf2((f32x2){sm[0], sm[1]}) * rsel), yb = rbf2(rbf2((f32x2){sm[2], sm[3]}) * rsel);
            u32x2 o;
            o.x = pkbf(unpk(h01) + rbf2(unpk(z01) + ya));
            o.y = pkbf(unpk(h23) + rbf2(unpk(z23) + yb));
            if (lane == 0) *(u32x2*)(out + (size_t)row * D + cq * 4) = o;
            STAMP(15);
        }
    }
    STAMP_END();
}

extern "C" void kernel_launch(void* const* d_in, const int* in_sizes, int n_in, void* d_out, int out_size,
                              void* d_ws, size_t ws_size, hipStream_t stream) {
    (void)hipFuncSetAttribute((const void*)fused_kernel, hipFuncAttributeMaxDynamicSharedMemorySize, LDS_BYTES);
    fused_kernel<<<256, 1024, LDS_BYTES, stream>>>(
        (const u16*)d_in[0], (const u16*)d_in[1], (const u16*)d_in[2], (const u16*)d_in[3], (const u16*)d_in[4],
        (const u16*)d_in[5], (const u16*)d_in[6], (const u16*)d_in[8], (const u16*)d_in[9], (const u16*)d_in[10],
        (const u16*)d_in[11], (u16*)d_out, (char*)d_ws);
}
```

```cpp
#define FASTR 1
#include <hip/hip_runtime.h>
#include <hip/hip_bf16.h>
#include <stdint.h>

typedef _Float16 f16;
typedef f16 f16x2 __attribute__((ext_vector_type(2)));
typedef f16 f16x4 __attribute__((ext_vector_type(4)));
typedef f16 f16x8 __attribute__((ext_vector_type(8)));
typedef short s16x4 __attribute__((ext_vector_type(4)));
typedef __bf16 bf16x2 __attribute__((ext_vector_type(2)));
typedef __bf16 bf16x8 __attribute__((ext_vector_type(8)));
typedef float f32x2 __attribute__((ext_vector_type(2)));
typedef float f32x4 __attribute__((ext_vector_type(4)));
typedef float f32x16 __attribute__((ext_vector_type(16)));
typedef unsigned u32x2 __attribute__((ext_vector_type(2)));
typedef unsigned u32x4 __attribute__((ext_vector_type(4)));
typedef unsigned short u16;
typedef unsigned long long u64;
#define LAS __attribute__((address_space(3)))

constexpr int L = 2048, D = 640, NM = 128, F = 1728;

#define ST_ARG
#define STAMPC(i)
#define ST_PASS
#define STAMP_DECL
#define STAMP_END()
#define STAMP(i)

#define BAR() asm volatile("s_waitcnt lgkmcnt(0)\n\ts_barrier" ::: "memory")
#define BAR_NOWAIT() asm volatile("s_barrier" ::: "memory")
#define DRAIN() asm volatile("s_waitcnt vmcnt(0)" ::: "memory")
#define SB() __builtin_amdgcn_sched_barrier(0)

__device__ __forceinline__ float bf2f(unsigned x) { return __uint_as_float(x << 16); }
__device__ __forceinline__ f32x2 unpk(unsigned x) {
    f32x2 r; r.x = __uint_as_float(x << 16); r.y = __uint_as_float(x & 0xffff0000u); return r;
}
__device__ __forceinline__ float rbf(float x) { return (float)(__bf16)x; }
__device__ __forceinline__ unsigned f2bf(float x) { __bf16 b = (__bf16)x; return (unsigned)__builtin_bit_cast(u16, b); }
__device__ __forceinline__ unsigned pkbf(f32x2 v) {
    bf16x2 b = __builtin_convertvector(v, bf16x2); return __builtin_bit_cast(unsigned, b);
}
__device__ __forceinline__ f32x2 rbf2(f32x2 v) { return unpk(pkbf(v)); }
__device__ __forceinline__ unsigned pkh(float lo, float hi) {
    f32x2 v = {lo, hi}; f16x2 h = __builtin_convertvector(v, f16x2); return __builtin_bit_cast(unsigned, h);
}
__device__ __forceinline__ unsigned padd(unsigned a, unsigned b) {
    return __builtin_bit_cast(unsigned, __builtin_bit_cast(f16x2, a) + __builtin_bit_cast(f16x2, b));
}
__device__ __forceinline__ float dot2bf(unsigned a, unsigned b, float acc) {
    return __builtin_amdgcn_fdot2_f32_bf16(__builtin_bit_cast(bf16x2, a), __builtin_bit_cast(bf16x2, b), acc, false);
}
__device__ __forceinline__ f16x4 trread(const char* p) {
    return __builtin_bit_cast(f16x4, __builtin_amdgcn_ds_read_tr16_b64_v4i16((LAS s16x4*)p));
}
__device__ __forceinline__ f16x8 cat(f16x4 a, f16x4 b) { return __builtin_shufflevector(a, b, 0, 1, 2, 3, 4, 5, 6, 7); }

template <int CTRL, int RM = 0xf>
__device__ __forceinline__ float dppf(float v) {
    return __builtin_bit_cast(float, __builtin_amdgcn_update_dpp(0, __builtin_bit_cast(int, v), CTRL, RM, 0xf, false));
}
template <int CTRL, int RM = 0xf>
__device__ __forceinline__ unsigned dppu(unsigned v) {
    return (unsigned)__builtin_amdgcn_update_dpp(0, (int)v, CTRL, RM, 0xf, false);
}
__device__ __forceinline__ float row16_sum(float v) {
    v += dppf<0xB1>(v); v += dppf<0x4E>(v); v += dppf<0x141>(v); v += dppf<0x140>(v);
    return v;
}
__device__ __forceinline__ float wave_sum_u(float v) {
    v = row16_sum(v);
    v += dppf<0x142, 0xa>(v);
    v += dppf<0x143, 0xc>(v);
    return __builtin_bit_cast(float, __builtin_amdgcn_readlane(__builtin_bit_cast(int, v), 63));
}
__device__ __forceinline__ unsigned wave_max_u(unsigned v) {
    v = max(v, dppu<0xB1>(v)); v = max(v, dppu<0x4E>(v)); v = max(v, dppu<0x141>(v)); v = max(v, dppu<0x140>(v));
    v = max(v, dppu<0x142, 0xa>(v)); v = max(v, dppu<0x143, 0xc>(v));
    return (unsigned)__builtin_amdgcn_readlane((int)v, 63);
}
__device__ __forceinline__ f32x2 cmul(f32x2 a, f32x2 b) {
    f32x2 o;
    asm("v_pk_mul_f32 %0, %1, %2 op_sel_hi:[0,1]\n\t"
        "v_pk_fma_f32 %0, %1, %2, %0 op_sel:[1,1,0] op_sel_hi:[1,0,1] neg_lo:[0,1,0]"
        : "=&v"(o) : "v"(a), "v"(b));
    return o;
}
__device__ __forceinline__ float exp_acc(float x) {
    const float l2e = 1.44269504088896341f, l2e_lo = 1.92596299e-8f;
    const float t = x * l2e;
    float e = fmaf(x, l2e, -t);
    e = fmaf(x, l2e_lo, e);
    const float r = __builtin_amdgcn_exp2f(t);
    return fmaf(r * e, 0.693147180559945f, r);
}
__device__ __forceinline__ float sigmoid_f(float x) {
    const float e = __builtin_amdgcn_exp2f(fminf(-x * 1.44269504088896341f, 126.f));
    return __builtin_amdgcn_rcpf(1.f + e);
}
__device__ __forceinline__ f32x2 cexp_p(float zl, float wh, float wl, float p) {
    const float mag = __builtin_amdgcn_exp2f(zl * p);
    float t = __builtin_amdgcn_fractf(wh * p);
    t = fmaf(wl, p, t);
    f32x2 o = {__builtin_amdgcn_cosf(t), __builtin_amdgcn_sinf(t)};
    return o * mag;
}
__device__ __forceinline__ void ln_finish(float piv, float s1, float s2, float& mean_b, float& rstd) {
    const float dm = s1 * (1.f / D);
    const float var = fmaxf(fmaf(-dm, dm, s2 * (1.f / D)), 0.f);
    mean_b = rbf(piv + dm);
    rstd = __builtin_amdgcn_rcpf(rbf(__builtin_amdgcn_sqrtf(rbf(rbf(var) + 1e-5f))));
}
__device__ __forceinline__ void row_stats(const f32x2 (&v)[5], float& mb, float& rs) {
    const float piv = __builtin_bit_cast(float, __builtin_amdgcn_readfirstlane(__builtin_bit_cast(int, v[0].x)));
    f32x2 s1 = {0.f, 0.f}, s2 = {0.f, 0.f};
#pragma unroll
    for (int i = 0; i < 5; ++i) { f32x2 d = v[i] - piv; s1 += d; s2 = __builtin_elementwise_fma(d, d, s2); }
    const float t1 = wave_sum_u(s1.x + s1.y), t2 = wave_sum_u(s2.x + s2.y);
    ln_finish(piv, t1, t2, mb, rs);
}

constexpr unsigned WS_EPOCH = 0;
#define FSTR_ 64
constexpr unsigned FSTR = FSTR_;
constexpr unsigned WS_FLAGS = 4096;
constexpr unsigned WS_FLAGS2 = WS_FLAGS + 256 * FSTR;
constexpr unsigned WS_KEYS = WS_FLAGS2 + 256 * FSTR;
constexpr unsigned WS_REC = 131072;
constexpr unsigned WS_Y = 2u << 20;
constexpr unsigned WS_UT = 5u << 20;
constexpr unsigned WS_END = 8u << 20;
constexpr unsigned SENT = 0xffffffffu;
constexpr int SPIN_MAX = 20000;

constexpr int MSTR = 272, TABSZ = NM * MSTR;
constexpr int WSTR = 3200, WSLOT = 4 * WSTR;
constexpr int WG = 3 * WSLOT;
constexpr int KB = 3 * TABSZ, KCS = 1344, T2 = 4 * KCS, T3 = T2 + 79 * 16, KSLOT = T3 + 256;
constexpr int UB = KB + 3 * KSLOT, UOFF = 17 * 80, USLOT = 81 * 80;
constexpr int TRB = UB + 3 * USLOT, RSTR = 1344;
constexpr int MISC = TRB + 8 * RSTR;
constexpr int LDS_BYTES = MISC + 1024;
static_assert(WG + 40960 <= KB && LDS_BYTES <= 163840 && KSLOT % 16 == 0, "lds map");

typedef __amdgpu_buffer_rsrc_t rsrc_t;
__device__ __forceinline__ void st16(rsrc_t r, unsigned off, u32x4 v) { __builtin_amdgcn_raw_buffer_store_b128(v, r, off, 0, 16); }
__device__ __forceinline__ void st8(rsrc_t r, unsigned off, u32x2 v) { __builtin_amdgcn_raw_buffer_store_b64(v, r, off, 0, 16); }
__device__ __forceinline__ void st4(rsrc_t r, unsigned off, unsigned v) { __builtin_amdgcn_raw_buffer_store_b32(v, r, off, 0, 16); }
__device__ __forceinline__ u32x4 ld16(rsrc_t r, unsigned off) { return __builtin_amdgcn_raw_buffer_load_b128(r, off, 0, 16); }
__device__ __forceinline__ u32x2 ld8(rsrc_t r, unsigned off) { return __builtin_amdgcn_raw_buffer_load_b64(r, off, 0, 16); }
__device__ __forceinline__ unsigned ld4(rsrc_t r, unsigned off) { return __builtin_amdgcn_raw_buffer_load_b32(r, off, 0, 16); }
__device__ __forceinline__ bool pending(u32x4 v) { return max(max(v.x, v.y), max(v.z, v.w)) == SENT; }
__device__ __forceinline__ u32x4 poll16(rsrc_t r, unsigned off, u32x4 v) {
    for (int s = 0; s < SPIN_MAX; ++s) {
        const bool pend = pending(v);
        if (!__any(pend)) break;
        __builtin_amdgcn_s_sleep(2);
        asm volatile("" ::: "memory");
        if (pend) v = ld16(r, off);
    }
    return v;
}
template <typename BAD>
__device__ __forceinline__ void sweep4(rsrc_t r, unsigned base, int lane, unsigned (&f)[4], BAD bad) {
    const unsigned off = base + lane * FSTR;
#pragma unroll
    for (int k = 0; k < 4; ++k) f[k] = ld4(r, off + k * 64 * FSTR);
    for (int s = 0; s < SPIN_MAX; ++s) {
        if (!__any(bad(f[0]) || bad(f[1]) || bad(f[2]) || bad(f[3]))) break;
        __builtin_amdgcn_s_sleep(2);
        asm volatile("" ::: "memory");
#pragma unroll
        for (int k = 0; k < 4; ++k) if (bad(f[k])) f[k] = ld4(r, off + k * 64 * FSTR);
    }
}
__device__ __forceinline__ unsigned poll_keys(rsrc_t r, int lane) {
    unsigned f[4];
    sweep4(r, WS_KEYS, lane, f, [](unsigned v) { return v == SENT; });
    return wave_max_u(max(max(f[0], f[1]), max(f[2], f[3])));
}

#define MFMA32(a, b, c) __builtin_amdgcn_mfma_f32_32x32x16_f16(a, b, c, 0, 0, 0)

struct ConvPtr { const char *an, *bn, *a2, *b2, *a3, *b3; };
template <int TILE> struct Steps {
    static constexpr int NN = TILE ? 38 : 35, N2 = TILE ? 24 : 8, N3 = TILE ? 8 : 0, N = NN + N2 + N3;
};
template <int TILE, int S>
__device__ __forceinline__ void conv_ld(const ConvPtr& P, f16x8& A, f16x8& B) {
    using St = Steps<TILE>;
    if constexpr (S < St::NN) {
        constexpr int n = S - 1, nb = n > 33 ? n + 29 : n;
        constexpr int boff = (TILE ? 33 : 17) * 80 + ((nb & 1) ? -((nb + 1) / 2) * 80 + 32 : -(nb / 2) * 80);
        A = cat(trread(P.an + S * 32 + 8), trread(P.an + S * 32));
        B = *(const f16x8*)(P.bn + boff);
    } else if constexpr (S < St::NN + St::N2) {
        constexpr int s = S - St::NN;
        A = cat(trread(P.a2 + s * 128 + 32), trread(P.a2 + s * 128));
        B = *(const f16x8*)(P.b2 + (St::N2 - 1 - s) * 32);
    } else {
        constexpr int s = S - St::NN - St::N2;
        A = cat(trread(P.a3 + s * 256), trread(P.a3 + s * 256 + 64));
        B = cat(*(const f16x4*)(P.b3 + (7 - s) * 32 + 8), *(const f16x4*)(P.b3 + (7 - s) * 32));
    }
}
constexpr int PF = 4;
template <int TILE, int S>
__device__ __forceinline__ void conv_steps(const ConvPtr& P, f16x8 (&A)[PF], f16x8 (&B)[PF], f32x16& acc,
                                           volatile LAS unsigned* cnt, unsigned& seen ST_ARG) {
    using St = Steps<TILE>;
    if constexpr (S < St::N) {
        acc = MFMA32(A[S % PF], B[S % PF], acc);
        if constexpr (S + PF + 4 == St::NN) seen = *cnt;
        if constexpr (S + PF == St::NN) {
            SB();
            for (int s = 0; s < SPIN_MAX && __builtin_amdgcn_readfirstlane(seen) < 8u; ++s) seen = *cnt;
            asm volatile("" ::: "memory");
        }
        if constexpr (S + PF < St::N) conv_ld<TILE, S + PF>(P, A[S % PF], B[S % PF]);
        SB();
        conv_steps<TILE, S + 1>(P, A, B, acc, cnt, seen ST_PASS);
    }
}
__device__ __forceinline__ ConvPtr conv_setup(char* smem, int slot, int tile, int lane) {
    const int c = lane & 31, h = lane >> 5, q = (lane >> 2) & 3;
    const int G = ((lane >> 4) & 1) * 4 + (lane & 3);
    const int tr = 16 * (G & 1) + 4 * (G >> 1);
    const char* wsl = smem + slot * WSLOT;
    const char* ks = smem + KB + slot * KSLOT;
    ConvPtr P;
    P.an = ks + q * KCS + 2 * (tr - 8 * h + 8);
    P.bn = smem + UB + slot * USLOT + UOFF + (c - (tile ? 1 : 17)) * 80 + 16 * h;
    P.a2 = wsl + (q & 1) * WSTR + 2 * (36 + 32 * h + tr - 4 * (q + 1) - 16);
    P.b2 = ks + T2 + (1 + c - h) * 16;
    P.a3 = wsl + (2 + (q & 1)) * WSTR + 2 * (36 + 64 * h + tr - 8 * (q + 1));
    P.b3 = ks + T3 + (c - 2 * h - 29) * 8;
    return P;
}
template <int TILE>
__device__ __forceinline__ void conv_tile(char* smem, const ConvPtr& P, int slot, unsigned yo, rsrc_t wsr ST_ARG) {
    f16x8 A[PF], B[PF];
    conv_ld<TILE, 0>(P, A[0], B[0]); conv_ld<TILE, 1>(P, A[1], B[1]);
    conv_ld<TILE, 2>(P, A[2], B[2]); conv_ld<TILE, 3>(P, A[3], B[3]);
    const float sc = *(const float*)(smem + MISC + 96 + slot * 4);
    f32x16 acc;
#pragma unroll
    for (int i = 0; i < 16; ++i) acc[i] = 0.f;
    SB();
    unsigned seen = 0;
    conv_steps<TILE, 0>(P, A, B, acc, (volatile LAS unsigned*)(smem + MISC + 48), seen ST_PASS);
#pragma unroll
    for (int g = 0; g < 2; ++g) {
        u32x4 o;
#pragma unroll
        for (int e = 0; e < 4; ++e) o[e] = pkbf((f32x2){acc[8 * g + 2 * e], acc[8 * g + 2 * e + 1]} * sc);
        st16(wsr, yo + g * 1024, o);
    }
}

template <bool FULL>
__device__ __forceinline__ void conv_windows(char* smem, int slot, int i, int lane) {
    const char* op = smem + UB + slot * USLOT + UOFF + (lane >> 2) * 80 + (lane & 3) * 16 + i * 1280;
    const u32x4 own = *(const u32x4*)op;
    const u32x4 pv = *(const u32x4*)(op - ((lane & 3) ? 16 : 32));
    unsigned w[8] = {pv.x, pv.y, pv.z, pv.w, own.x, own.y, own.z, own.w};
    unsigned a[7];
#pragma unroll
    for (int k = 0; k < 7; ++k) a[k] = __builtin_amdgcn_alignbit(w[k + 1], w[k], 16);
    u32x4 w3, w5, w7, w9;
#pragma unroll
    for (int k = 0; k < 4; ++k) {
        w3[k] = padd(w[2 + k], padd(a[1 + k], a[2 + k]));
        w5[k] = padd(w3[k], padd(w[1 + k], w[3 + k]));
        if (FULL) {
            w7[k] = padd(w5[k], padd(a[k], a[3 + k]));
            w9[k] = padd(w7[k], padd(w[k], w[4 + k]));
        }
    }
    char* wp = smem + slot * WSLOT + 64 + lane * 16 + i * 1024;
    *(u32x4*)(wp) = w3;
    *(u32x4*)(wp + WSTR) = w5;
    if (FULL) {
        *(u32x4*)(wp + 2 * WSTR) = w7;
        *(u32x4*)(wp + 3 * WSTR) = w9;
    }
}

#define SLP_FLAG 6
#define SLP_F2 16
#define SLP_CLR 12
#define SLP_KEYS 16
#define SLP_W 12
__device__ __forceinline__ void lds_add1(char* q) {
    __hip_atomic_fetch_add((LAS unsigned*)q, 1u, __ATOMIC_RELAXED, __HIP_MEMORY_SCOPE_WORKGROUP);
}

__device__ __forceinline__ void lds_wait(char* q, unsigned n) {
    for (int s = 0; s < SPIN_MAX; ++s) {
        if (__builtin_amdgcn_readfirstlane(*(volatile LAS unsigned*)q) >= n) break;
        __builtin_amdgcn_s_sleep(1);
    }
    asm volatile("" ::: "memory");
}
#define LGKM0() asm volatile("s_waitcnt lgkmcnt(0)" ::: "memory")
#define RB2(v) (v)

extern "C" __global__ void __launch_bounds__(1024, 4)
fused_kernel(const u16* __restrict__ x, const u16* __restrict__ logA, const u16* __restrict__ Aim,
             const u16* __restrict__ Cre, const u16* __restrict__ Cim, const u16* __restrict__ logdt,
             const u16* __restrict__ dskip, const u16* __restrict__ wr, const u16* __restrict__ wg,
             const u16* __restrict__ wu, const u16* __restrict__ wd, u16* out, char* ws) {
    extern __shared__ __attribute__((aligned(16))) char smem[];
    asm volatile("" :: "s"(x), "s"(logA), "s"(Aim), "s"(Cre), "s"(Cim), "s"(logdt), "s"(dskip), "s"(wr), "s"(wg), "s"(wu),
                 "s"(wd), "s"(out), "s"(ws));
    const int tid = threadIdx.x, lane = tid & 63;
    const int wave = __builtin_amdgcn_readfirstlane(tid >> 6);
    const int p = (blockIdx.x & 7) * 32 + (blockIdx.x >> 3);
    const int isB = p & 1;
    const int d0 = 5 * (p >> 1) + 2 * isB;
    const int fs = (p >> 5) * 14 + (p & 31);
    const bool ffn = (p & 31) < 14 && fs < 108;
    const rsrc_t wsr = __builtin_amdgcn_make_buffer_rsrc(ws, 0, WS_END, 0x00020000);
    const u32x4 s4 = {SENT, SENT, SENT, SENT};
    STAMP_DECL; STAMP(0);

    auto ld5 = [&](const u16* q_, unsigned (&o)[5]) {
        u32x2 a = *(const u32x2*)(q_ + lane * 4), b = *(const u32x2*)(q_ + 256 + lane * 4);
        o[0] = a.x; o[1] = a.y; o[2] = b.x; o[3] = b.y; o[4] = *(const unsigned*)(q_ + 512 + lane * 2);
    };
    auto chof = [&](int s) { return isB ? d0 + (s == 2 ? 0 : s + 1) : d0 + s; };
    const int row = p * 8 + (wave & 7);
    const int sub = wave & 3;
    unsigned xr[5];
    unsigned tag = 0, cpf = 0;
    float mb1 = 0.f, rs1 = 0.f;
    if (wave < 8) {
        ld5(x + (size_t)row * D, xr);
        tag = ld4(wsr, WS_EPOCH);
        {
            u64 pc; unsigned off, len;
            asm volatile("s_getpc_b64 %0\n.Lfk_pc:" : "=s"(pc));
            asm volatile("s_mov_b32 %0, .Lfk_pc-fused_kernel\n\ts_mov_b32 %1, .Lfk_end-fused_kernel" : "=s"(off), "=s"(len));
            const rsrc_t cr = __builtin_amdgcn_make_buffer_rsrc((void*)(pc - off), 0, len, 0x00020000);
            cpf = __builtin_amdgcn_raw_buffer_load_b32(cr, tid * 128, 0, 0);
        }
        if (lane < 4) *(u32x4*)(smem + MISC + 32 + lane * 16) = (u32x4){0u, 0u, 0u, 0u};
        char* tp = smem + TRB + wave * RSTR + lane * 4;
        const char* tp2 = smem + TRB + ((lane >> 2) & 3) * RSTR + ((lane >> 4) * 16 + (lane & 3) * 4) * 2 + wave * 128;
        const unsigned uso = WS_UT + (p * D + tid) * 16;
        BAR();
        __builtin_amdgcn_s_setprio(3);
        f32x2 v[5];
#pragma unroll
        for (int i = 0; i < 5; ++i) v[i] = unpk(xr[i]);
        STAMP(1);
        row_stats(v, mb1, rs1);
        unsigned uh[5];
#pragma unroll
        for (int i = 0; i < 5; ++i) {
            const f32x2 u = RB2(RB2(v[i] - mb1) * rs1);
            uh[i] = pkh(u.x, u.y);
        }
        *(u32x2*)(tp + lane * 4) = (u32x2){uh[0], uh[1]};
        *(u32x2*)(tp + 512 + lane * 4) = (u32x2){uh[2], uh[3]};
        *(unsigned*)(tp + 1024) = uh[4];
        tag = __builtin_amdgcn_readfirstlane(tag) + 1u;
        asm volatile("s_waitcnt lgkmcnt(0)" : "+s"(tag) :: "memory");
        if (lane == 0) lds_add1(smem + MISC + 52);
        lds_wait(smem + MISC + 52, 9u);
        STAMP(2);
#pragma unroll
        for (int i = 0; i < 2; ++i) {
            if (i < 1 || wave < 2) {
                const f16x8 o = cat(trread(tp2 + i * 1024), trread(tp2 + i * 1024 + 4 * RSTR));
                st16(wsr, uso + i * 8192, __builtin_bit_cast(u32x4, o));
            }
        }
        __builtin_amdgcn_s_setprio(0);
        STAMP(3);
    } else {
        const int slot = wave < 12 ? 1 + (sub >> 1) : 0;
        const bool doA = wave < 14, doB = wave < 12 || wave >= 14;
        const int dch = chof(slot);
        if (wave == 15) {
#pragma unroll
            for (int i = 0; i < 10; ++i) st16(wsr, WS_UT + (p * D + i * 64 + lane) * 16, s4);
            tag = ld4(wsr, WS_EPOCH);
        }
        const unsigned ldt_ = ((const unsigned*)logdt)[dch >> 1];
        const unsigned la_ = ((const unsigned*)(logA + dch * NM))[lane];
        const unsigned ai_ = ((const unsigned*)(Aim + dch * NM))[lane];
        const unsigned c0 = ((const unsigned*)(Cre + dch * NM))[lane];
        const unsigned c1 = ((const unsigned*)(Cim + dch * NM))[lane];
        BAR();
        {
            const u32x4 z = {0u, 0u, 0u, 0u};
            const int zi = tid - 512;
#pragma unroll
            for (int i = 0; i < 3; ++i) if (i < 2 || zi < 3 * KSLOT / 16 - 1024) *(u32x4*)(smem + KB + (i * 512 + zi) * 16) = z;
            if (zi < 384 && (zi & 127) < 85) *(u32x4*)(smem + UB + (zi >> 7) * USLOT + (zi & 127) * 16) = z;
            LGKM0();
            if (lane == 0) lds_add1(smem + MISC + 56);
        }
        if (wave == 15) {
            DRAIN();
            if (lane == 0) { st4(wsr, WS_FLAGS + p * FSTR, tag + 1u); lds_add1(smem + MISC + 52); }
            __builtin_amdgcn_s_setprio(2);
            STAMP(3);
        }
        const unsigned sh = (wave & 1) ? 0u : 16u;
        auto pick = [&](unsigned w) { return __uint_as_float((w << sh) & 0xffff0000u); };
        const float dt = rbf(exp_acc(__uint_as_float((dch & 1) ? ldt_ & 0xffff0000u : ldt_ << 16)));
        const float zr = -rbf(exp_acc(pick(la_))) * dt;
        const float zi = pick(ai_) * dt;
        const float zl = zr * 1.44269504088896341f;
        const float wh = zi * 0.15915494309189535f;
        const float wl = fmaf(zi, 6.4206383e-09f, fmaf(zi, 0.15915494309189535f, -wh));
        STAMP(1);
        const f32x2 e8 = cexp_p(zl, wh, wl, 8.f);
        const f32x2 e128 = cexp_p(zl, wh, wl, 128.f);
        const f32x2 e256 = cmul(e128, e128), e384 = cmul(e128, e256), e512 = cmul(e256, e256);
        char* tb = smem + slot * TABSZ + ((wave & 1) * 64 + lane) * MSTR;
        auto put8 = [&](int grp, const f32x2 (&e)[8]) {
            u32x4 re = {pkh(e[0].x, e[1].x), pkh(e[2].x, e[3].x), pkh(e[4].x, e[5].x), pkh(e[6].x, e[7].x)};
            u32x4 im = {pkh(e[0].y, e[1].y), pkh(e[2].y, e[3].y), pkh(e[4].y, e[5].y), pkh(e[6].y, e[7].y)};
            *(u32x4*)(tb + grp * 16) = re;
            *(u32x4*)(tb + 128 + grp * 16) = im;
        };
        if (doA) {
            f32x2 e[8], g[8];
            e[0] = (f32x2){1.f, 0.f};
            e[1] = cexp_p(zl, wh, wl, 1.f);
            e[2] = cmul(e[1], e[1]); e[4] = cmul(e[2], e[2]);
            e[3] = cmul(e[1], e[2]); e[5] = cmul(e[1], e[4]); e[6] = cmul(e[2], e[4]); e[7] = cmul(e[3], e[4]);
            put8(0, e);
            g[0] = e128;
#pragma unroll
            for (int r = 1; r < 8; ++r) g[r] = cmul(e[r], e128);
            put8(1, g);
            g[0] = cmul(e[2], e256); g[1] = cmul(e[4], e256); g[2] = cmul(e[6], e256); g[3] = cmul(e8, e256);
            g[4] = cmul(e[2], e384); g[5] = cmul(e[4], e384); g[6] = cmul(e[6], e384); g[7] = cmul(e8, e384);
            put8(2, g);
            g[0] = cmul(e512, e[4]); g[1] = cmul(e512, e8);
            g[2] = cmul(g[0], e128); g[3] = cmul(g[1], e128);
            g[4] = cmul(g[0], e256); g[5] = cmul(g[1], e256);
            g[6] = cmul(g[0], e384); g[7] = cmul(g[1], e384);
            put8(3, g);
        }
        if (doB) {
            const unsigned m0 = c0 & 0x7fff7fffu, m1 = c1 & 0x7fff7fffu;
            const unsigned mx = wave_max_u(max(max(m0 >> 16, m0 & 0xffffu), max(m1 >> 16, m1 & 0xffffu)));
            const int ex = min(max((int)(mx >> 7), 8), 246);
            const float up = __uint_as_float((unsigned)(260 - ex) << 23);
            if (lane == 0) *(float*)(smem + MISC + 96 + slot * 4) = 2.f * dt * __uint_as_float((unsigned)(ex - 6) << 23);
            const f32x2 c8 = {e8.x, -e8.y};
            const f32x2 c16 = cmul(c8, c8), c32 = cmul(c16, c16), c64 = cmul(c32, c32);
            const f32x2 e1024 = cmul(e512, e512);
            f32x2 b[8], g[8];
            g[0] = cmul(e1024, e8); g[1] = cmul(g[0], e128); g[2] = cmul(g[0], e256); g[3] = cmul(g[0], e384);
#pragma unroll
            for (int s = 0; s < 4; ++s) g[4 + s] = cmul(g[s], e512);
            put8(4, g);
            *(u32x2*)(tb + 80) = (u32x2){pkh(e256.x, e512.x), pkh(e1024.x, 0.f)};
            *(u32x2*)(tb + 128 + 80) = (u32x2){pkh(e256.y, e512.y), pkh(e1024.y, 0.f)};
            b[0] = (f32x2){pick(c0) * up, -pick(c1) * up};
            b[1] = cmul(b[0], c8); b[2] = cmul(b[0], c16); b[3] = cmul(b[1], c16);
#pragma unroll
            for (int j = 0; j < 4; ++j) b[4 + j] = cmul(b[j], c32);
            put8(6, b);
#pragma unroll
            for (int j = 0; j < 8; ++j) g[j] = cmul(b[j], c64);
            put8(7, g);
        }
        LGKM0();
        if (lane == 0) lds_add1(smem + MISC + 64 + slot * 4);
        __builtin_amdgcn_s_setprio(0);
        STAMP(4);
    }
    if (wave >= 8 || wave == 3) {
        const int ts = wave == 3 ? 2 : (wave == 15 ? 1 : (wave < 12 ? 1 + (sub >> 1) : 0));
        const int tt = sub == 3 ? (wave == 11 ? 1 : 2) : (wave < 12 ? (sub & 1) : sub);
        char* ks = smem + KB + ts * KSLOT;
        const int qq = lane >> 4, q = (lane >> 2) & 3, pp = lane & 3, j = lane & 15;
        const char* ap = smem + ts * TABSZ + (4 * qq + 2 * (q >> 1)) * MSTR + (q & 1) * 128 + pp * 8;
        const char* bp = ap + (tt == 2 ? 64 : 96);
        ap += tt == 2 ? 96 : tt * 32;
        lds_wait(smem + MISC + 56, 8u);
        lds_wait(smem + MISC + 64 + ts * 4, ts ? 2u : 4u);
        STAMP(5);
        f16x8 av[8], bv[8];
#pragma unroll
        for (int k = 0; k < 8; ++k) {
            av[k] = cat(trread(ap + k * 16 * MSTR), trread(ap + k * 16 * MSTR + MSTR));
            bv[k] = cat(trread(bp + k * 16 * MSTR), trread(bp + k * 16 * MSTR + MSTR));
        }
        SB();
        f32x4 acc = {0.f, 0.f, 0.f, 0.f}, acc1 = {0.f, 0.f, 0.f, 0.f};
#pragma unroll
        for (int k = 0; k < 4; ++k) {
            acc = __builtin_amdgcn_mfma_f32_16x16x32_f16(av[2 * k], bv[2 * k], acc, 0, 0, 0);
            acc1 = __builtin_amdgcn_mfma_f32_16x16x32_f16(av[2 * k + 1], bv[2 * k + 1], acc1, 0, 0, 0);
        }
        acc += acc1;
        const unsigned p01 = pkh(acc[0], acc[1]), p23 = pkh(acc[2], acc[3]), p12 = pkh(acc[1], acc[2]);
        if (tt == 0) {
            char* o = ks + 2 * (28 + 4 * (qq & 1) + 128 * (qq >> 1) + 8 * j);
            *(u32x2*)o = (u32x2){p01, p23};
            *(unsigned*)(o + 2 * KCS + 4) = p01; *(unsigned*)(o + 2 * KCS + 8) = p23;
            *(u16*)(o + KCS + 2) = (u16)p01; *(unsigned*)(o + KCS + 4) = p12; *(u16*)(o + KCS + 8) = (u16)(p23 >> 16);
            *(u16*)(o + 3 * KCS + 6) = (u16)p01; *(unsigned*)(o + 3 * KCS + 8) = p12; *(u16*)(o + 3 * KCS + 12) = (u16)(p23 >> 16);
        } else if (tt == 1) {
            if (qq < 2) {
                const unsigned p11 = pkh(acc[1], acc[1]), p33 = pkh(acc[3], acc[3]);
                char* o = ks + 2 * (28 + 258 + 128 * qq + 8 * j);
                unsigned* o0 = (unsigned*)o;
                o0[0] = p01; o0[1] = p11; o0[2] = p23; o0[3] = p33;
                *(u32x4*)(o + 2 * KCS + 4) = (u32x4){p01, p11, p23, p33};
                *(u16*)(o + KCS + 2) = (u16)p01; *(u32x2*)(o + KCS + 4) = (u32x2){p11, p12};
                *(unsigned*)(o + KCS + 12) = p33; *(u16*)(o + KCS + 16) = (u16)p33;
                *(u16*)(o + 3 * KCS + 6) = (u16)p01; *(unsigned*)(o + 3 * KCS + 8) = p11;
                *(u32x2*)(o + 3 * KCS + 12) = (u32x2){p12, p33}; *(u16*)(o + 3 * KCS + 20) = (u16)p33;
            } else {
                unsigned* t2 = (unsigned*)(ks + T2 + 31 * 16) + 32 * (qq - 2) + j;
                t2[0] = p01; t2[16] = p23;
            }
        } else {
            if (j < 8) *(u32x2*)(ks + T3 + (16 * j + 4 * qq) * 2) = (u32x2){p01, p23};
            const unsigned s256 = __builtin_amdgcn_readlane((int)p01, 8), s512 = __builtin_amdgcn_readlane((int)p01, 9),
                           s1024 = __builtin_amdgcn_readlane((int)p01, 10);
            const int w = lane >> 2, cq = lane & 3;
            const int lg = w < 2 ? 256 + w : (w == 2 ? 514 : 560 + w);
            const unsigned sv = w < 2 ? s256 : (w == 2 ? s512 : s1024);
            if (w < 5) *(u16*)(ks + cq * KCS + 2 * (lg + cq + 28)) = (u16)sv;
        }
        if (wave >= 12) {
            const int xi = tid - 768;
            const int xg = p >> 5, li = (p & 31) * 160 + xi;
            const u16* src;
            if (xi < 160) src = ((li & 4) ? wu : wg) + (size_t)(li >> 3) * F + min(((7 * xg) >> 1) + (li & 3), 26) * 64;
            else src = wd + (size_t)min(xg * 2240 + (p & 31) * 70 + min(xi - 160, 69), F * 10 - 1) * 64;
            cpf = *(const unsigned*)src;
        }
        STAMP(6);
    } else if (wave < 3) {
        __builtin_amdgcn_s_sleep(SLP_CLR);
        if (wave) {
#pragma unroll
            for (int i = 0; i < 5; ++i) {
                const int ii = (wave - 1) * 5 + i;
                const int k = isB ? (ii < 2 ? ii : ii + 2) : (ii < 8 ? ii : ii + 2);
                st16(wsr, WS_Y + (d0 * 256 + k * 64 + lane) * 16, s4);
            }
        } else {
#pragma unroll
            for (int i = 0; i < 5; ++i) if (i < 3 || ffn) st16(wsr, WS_REC + p * 5120 + (i * 64 + lane) * 16, s4);
            if (lane == 0) st4(wsr, WS_KEYS + p * FSTR, SENT);
        }
        DRAIN();
        STAMP(6);
    } else {
        const unsigned fo = WS_FLAGS + (tid - 256) * FSTR;
        unsigned uo[3], lo[3];
#pragma unroll
        for (int i = 0; i < 3; ++i) {
            const int idx = i * 64 + lane, g0 = idx / 3, uc = idx - 3 * g0, ug = (wave - 4) * 64 + g0;
            const int us = isB ? (uc == 0 ? 2 : uc - 1) : uc;
            uo[i] = WS_UT + (ug * D + d0 + uc) * 16;
            lo[i] = UB + us * USLOT + UOFF + (ug >> 2) * 80 + (ug & 3) * 16;
        }
        unsigned f = ld4(wsr, fo);
        for (int s = 0; s < SPIN_MAX; ++s) {
            if (!__any(f != tag)) break;
            __builtin_amdgcn_s_sleep(2);
            asm volatile("" ::: "memory");
            if (f != tag) f = ld4(wsr, fo);
        }
        asm volatile("" ::: "memory");
        STAMP(4);
        u32x4 uv[3];
#pragma unroll
        for (int i = 0; i < 3; ++i) uv[i] = ld16(wsr, uo[i]);
#pragma unroll
        for (int i = 0; i < 3; ++i) {
            uv[i] = poll16(wsr, uo[i], uv[i]);
            *(u32x4*)(smem + lo[i]) = uv[i];
        }
        STAMP(6);
    }
    const int cslot = wave == 11 ? 0 : (wave == 15 ? 1 : wave - 8);
    const int ctile = (sub == 3 || (wave == 10 && isB)) ? 0 : 1;
    const ConvPtr CP = conv_setup(smem, cslot, ctile, lane);
    const unsigned cyo = WS_Y + (chof(cslot) * 256 + ctile * 128 + lane) * 16;
    STAMP(7);
    BAR();
    STAMP(8);
    unsigned wdp[16], er[5], wv[5];
    if (wave < 8) {
        ld5(dskip, er);
        ld5(wr, wv);
        if (tid == 448) {
            st4(wsr, WS_FLAGS2 + p * FSTR, tag);
            if (p == 0) st4(wsr, WS_EPOCH, tag);
        }
        if (wave < 6) conv_windows<true>(smem, wave >> 1, wave & 1, lane);
        else {
            conv_windows<false>(smem, wave - 6, 2, lane);
            if (wave == 6) conv_windows<false>(smem, 2, 2, lane);
            else if (lane < 48) *(u32x4*)(smem + (lane >> 2) * WSTR + (lane & 3) * 16) = (u32x4){0u, 0u, 0u, 0u};
        }
        LGKM0();
        if (lane == 0) lds_add1(smem + MISC + 48);
        STAMP(9);
#pragma unroll
        for (int i = 0; i < 5; ++i) er[i] = pkbf(RB2(RB2(unpk(xr[i]) - mb1) * rs1) * unpk(er[i]));
        if (ffn && wave < 5) {
            const unsigned* wp = (const unsigned*)(wd + (size_t)fs * 16 * D) + tid;
#pragma unroll
            for (int i = 0; i < 16; ++i) wdp[i] = wp[i * (D / 2)];
        }
        if (wave == 7) {
            __builtin_amdgcn_s_sleep(SLP_F2);
            unsigned f[4];
            sweep4(wsr, WS_FLAGS2, lane, f, [&](unsigned v) { return v != tag; });
        }
        STAMP(5);
    } else if (wave < 12 || wave == 15) {
        if (wave < 12) STAMPC(2);
        if (ctile) conv_tile<1>(smem, CP, cslot, cyo, wsr ST_PASS);
        else conv_tile<0>(smem, CP, cslot, cyo, wsr ST_PASS);
        if (wave < 12) STAMPC(3);
        STAMP(9);
        __builtin_amdgcn_s_setprio(0);
        DRAIN();
    } else if (ffn) {
        u32x4 wq[14];
        const int xi = tid - 768;
#pragma unroll
        for (int i = 0; i < 14; ++i) {
            const int ci = min(i * 192 + xi, 2559), r_ = ci >> 1;
            const u16* wsrc = r_ < D ? wg + (size_t)r_ * F : wu + (size_t)(r_ - D) * F;
            if (i < 13 || wave == 12) wq[i] = *(const u32x4*)(wsrc + fs * 16 + (ci & 1) * 8);
        }
        SB();
#pragma unroll
        for (int i = 0; i < 14; ++i) if (i < 13 || wave == 12) *(u32x4*)(smem + WG + (i * 192 + xi) * 16) = wq[i];
        STAMP(9);
    }
    asm volatile("" :: "v"(cpf));
    BAR();
    STAMP(10);
    if (wave < 10) {
        const unsigned yoff = WS_Y + (tid * 256 + (p >> 7) * 128 + (p & 1) * 64 + ((p >> 1) & 1) * 32 + ((p >> 2) & 31)) * 16;
        const u32x4 yv = poll16(wsr, yoff, ld16(wsr, yoff));
        u16* yp = (u16*)(smem + TRB) + tid;
#pragma unroll
        for (int i = 0; i < 4; ++i) {
            yp[(2 * i) * D] = (u16)yv[i];
            yp[(2 * i + 1) * D] = (u16)(yv[i] >> 16);
        }
    }
    STAMP(11);
    BAR();
    unsigned op[5];
    bool plain_out = false;
    if (wave < 8) {
        unsigned yr[5], hp[5], zp[5];
        ld5((const u16*)(smem + TRB) + wave * D, yr);
        f32x2 hv[5];
#pragma unroll
        for (int i = 0; i < 5; ++i) {
            hp[i] = pkbf(unpk(xr[i]) + RB2(unpk(yr[i]) + unpk(er[i])));
            hv[i] = unpk(hp[i]);
        }
        float mb, rs;
        row_stats(hv, mb, rs);
        float ra = 0.f;
#pragma unroll
        for (int i = 0; i < 5; ++i) {
            zp[i] = pkbf(RB2(hv[i] - mb) * rs);
            ra = dot2bf(zp[i], wv[i], ra);
        }
        const float r = wave_sum_u(ra);
        unsigned rb = f2bf(r);
        rb = (rb & 0x8000u) ? (~rb & 0xffffu) : (rb | 0x8000u);
        const unsigned key = (rb << 16) | (unsigned)(2047 - row);
        if (lane == 0) *(unsigned*)(smem + MISC + wave * 4) = key;
        BAR();
        STAMP(12);
        const u32x4 k0 = *(const u32x4*)(smem + MISC), k1 = *(const u32x4*)(smem + MISC + 16);
        const unsigned best = max(max(max(k0.x, k0.y), max(k0.z, k0.w)), max(max(k1.x, k1.y), max(k1.z, k1.w)));
        plain_out = __builtin_amdgcn_readfirstlane(best) != key;
        if (!plain_out) {
            const unsigned rec = WS_REC + p * 5120;
            if (lane == 0) st4(wsr, WS_KEYS + p * FSTR, key);
            st8(wsr, rec + lane * 8, (u32x2){zp[0], zp[1]});
            st8(wsr, rec + 512 + lane * 8, (u32x2){zp[2], zp[3]});
            st4(wsr, rec + 1024 + lane * 4, zp[4]);
        }
#pragma unroll
        for (int i = 0; i < 5; ++i) op[i] = pkbf(hv[i] + unpk(zp[i]));
        if (!plain_out) {
            const unsigned rec = WS_REC + p * 5120;
            const rsrc_t orr = __builtin_amdgcn_make_buffer_rsrc(out + (size_t)row * D, 0, D * 2, 0x00020000);
            st8(orr, lane * 8, (u32x2){op[0], op[1]});
            st8(orr, 512 + lane * 8, (u32x2){op[2], op[3]});
            st4(orr, 1024 + lane * 4, op[4]);
            DRAIN();
            st8(wsr, rec + 1280 + lane * 8, (u32x2){hp[0], hp[1]});
            st8(wsr, rec + 1280 + 512 + lane * 8, (u32x2){hp[2], hp[3]});
            st4(wsr, rec + 1280 + 1024 + lane * 4, hp[4]);
        }
    } else {
        BAR();
        STAMP(12);
    }
    if (ffn) {
        f16x8 bw[4];
        const int mat = wave >= 5, kgp = wave - 5 * mat;
        if (wave < 10) {
            const char* bp = smem + WG + mat * 20480 + (kgp * 128 + (lane >> 4) * 8 + ((lane >> 2) & 3)) * 32 + (lane & 3) * 8;
#pragma unroll
            for (int k = 0; k < 4; ++k) bw[k] = cat(trread(bp + k * 1024), trread(bp + k * 1024 + 128));
        } else if (wave == 15) {
            __builtin_amdgcn_s_sleep(SLP_KEYS);
            const unsigned bk = poll_keys(wsr, lane);
            if (lane == 0) *(unsigned*)(smem + MISC + 32) = bk;
        }
        unsigned wq[16];
        if (wave < 5) {
#pragma unroll
            for (int i = 0; i < 8; ++i) {
                wq[2 * i] = __builtin_amdgcn_perm(wdp[2 * i + 1], wdp[2 * i], 0x05040100u);
                wq[2 * i + 1] = __builtin_amdgcn_perm(wdp[2 * i + 1], wdp[2 * i], 0x07060302u);
            }
        }
        BAR();
        STAMP(13);
        if (wave < 10) {
            const unsigned bk = *(const unsigned*)(smem + MISC + 32);
            const unsigned xo = WS_REC + ((2047u - (bk & 2047u)) >> 3) * 5120 + (kgp * 128 + (lane >> 4) * 8) * 2;
            u32x4 xa[4];
#pragma unroll
            for (int k = 0; k < 4; ++k) xa[k] = ld16(wsr, xo + k * 64);
#pragma unroll
            for (int k = 0; k < 4; ++k) xa[k] = poll16(wsr, xo + k * 64, xa[k]);
            f32x4 acc = {0.f, 0.f, 0.f, 0.f};
#pragma unroll
            for (int k = 0; k < 4; ++k)
                acc = __builtin_amdgcn_mfma_f32_16x16x32_bf16(__builtin_bit_cast(bf16x8, xa[k]), __builtin_bit_cast(bf16x8, bw[k]), acc, 0, 0, 0);
            if (lane < 16) *(float*)(smem + MISC + 128 + (wave * 16 + lane) * 4) = acc[0];
        }
        BAR();
        STAMP(14);
        if (wave < 5) {
            const float* pr = (const float*)(smem + MISC + 128) + (lane & 15);
            const float g = rbf(pr[0] + pr[16] + pr[32] + pr[48] + pr[64]);
            const float u = rbf(pr[80] + pr[96] + pr[112] + pr[128] + pr[144]);
            const unsigned a = f2bf(rbf(g * sigmoid_f(g)) * u);
            const unsigned ap = a | (dppu<0xB1>(a) << 16);
            float y0 = 0.f, y1 = 0.f;
#pragma unroll
            for (int i = 0; i < 8; ++i) {
                const unsigned s = (unsigned)__builtin_amdgcn_readlane((int)ap, 2 * i);
                y0 = dot2bf(wq[2 * i], s, y0);
                y1 = dot2bf(wq[2 * i + 1], s, y1);
            }
            st8(wsr, WS_REC + p * 5120 + 2560 + tid * 8, (u32x2){__float_as_uint(y0), __float_as_uint(y1)});
        }
        STAMP(15);
    } else if (wave < 8) {
        for (int i = 0; i <= wave; ++i) __builtin_amdgcn_s_sleep(6);
    } else if ((p & 31) >= 16 && (p & 31) < 26 && wave >= 14) {
        const int cq = 2 * ((p >> 5) * 10 + (p & 31) - 16) + (wave - 14);
        __builtin_amdgcn_s_sleep(40);
        const unsigned bk = poll_keys(wsr, lane);
        const int wrow = 2047 - (int)(bk & 2047u);
        const unsigned zo = WS_REC + (wrow >> 3) * 5120 + cq * 8 + (lane & 1) * 1280;
        u32x2 zh = ld8(wsr, zo);
        const int f0 = min(lane, 53), f1 = f0 + 54;
        const unsigned po0 = WS_REC + ((f0 / 14) * 32 + f0 % 14) * 5120 + 2560 + cq * 16;
        const unsigned po1 = WS_REC + ((f1 / 14) * 32 + f1 % 14) * 5120 + 2560 + cq * 16;
        for (int s = 0; s < SPIN_MAX; ++s) {
            const bool pend = max(zh.x, zh.y) == SENT;
            if (!__any(pend)) break;
            __builtin_amdgcn_s_sleep(2);
            asm volatile("" ::: "memory");
            if (pend) zh = ld8(wsr, zo);
        }
        unsigned rb = bk >> 16;
        rb = (rb & 0x8000u) ? (rb & 0x7fffu) : (~rb & 0xffffu);
        const float rsel = rbf(sigmoid_f(bf2f(rb)));
        STAMP(13);
        u32x4 v0 = ld16(wsr, po0), v1 = ld16(wsr, po1);
        v0 = poll16(wsr, po0, v0);
        v1 = poll16(wsr, po1, v1);
        STAMP(14);
        f32x4 sv = __builtin_bit_cast(f32x4, v0) + __builtin_bit_cast(f32x4, v1);
        if (lane >= 54) sv = (f32x4){0.f, 0.f, 0.f, 0.f};
        float sm[4];
#pragma unroll
        for (int e = 0; e < 4; ++e) sm[e] = wave_sum_u(sv[e]);
        const unsigned z01 = __builtin_amdgcn_readlane((int)zh.x, 0), z23 = __builtin_amdgcn_readlane((int)zh.y, 0);
        const unsigned h01 = __builtin_amdgcn_readlane((int)zh.x, 1), h23 = __builtin_amdgcn_readlane((int)zh.y, 1);
        const f32x2 ya = rbf2(rbf2((f32x2){sm[0], sm[1]}) * rsel), yb = rbf2(rbf2((f32x2){sm[2], sm[3]}) * rsel);
        u32x2 o;
        o.x = pkbf(unpk(h01) + rbf2(unpk(z01) + ya));
        o.y = pkbf(unpk(h23) + rbf2(unpk(z23) + yb));
        if (lane == 0) *(u32x2*)(out + (size_t)wrow * D + cq * 4) = o;
        STAMP(11);
    }
    if (plain_out) {
        u16* q_ = out + (size_t)row * D;
        *(u32x2*)(q_ + lane * 4) = (u32x2){op[0], op[1]};
        *(u32x2*)(q_ + 256 + lane * 4) = (u32x2){op[2], op[3]};
        *(unsigned*)(q_ + 512 + lane * 2) = op[4];
    }
    STAMP_END();
    asm volatile(".Lfk_end:");
}

extern "C" void kernel_launch(void* const* d_in, const int* in_sizes, int n_in, void* d_out, int out_size,
                              void* d_ws, size_t ws_size, hipStream_t stream) {
    (void)hipFuncSetAttribute((const void*)fused_kernel, hipFuncAttributeMaxDynamicSharedMemorySize, LDS_BYTES);
    fused_kernel<<<256, 1024, LDS_BYTES, stream>>>(
        (const u16*)d_in[0], (const u16*)d_in[1], (const u16*)d_in[2], (const u16*)d_in[3], (const u16*)d_in[4],
        (const u16*)d_in[5], (const u16*)d_in[6], (const u16*)d_in[8], (const u16*)d_in[9], (const u16*)d_in[10],
        (const u16*)d_in[11], (u16*)d_out, (char*)d_ws);
}
```

```cpp
#define AMPK 0
#define AMPW 0
#include <hip/hip_runtime.h>
#include <hip/hip_bf16.h>
#include <stdint.h>

typedef _Float16 f16;
typedef f16 f16x2 __attribute__((ext_vector_type(2)));
typedef f16 f16x4 __attribute__((ext_vector_type(4)));
typedef f16 f16x8 __attribute__((ext_vector_type(8)));
typedef short s16x4 __attribute__((ext_vector_type(4)));
typedef __bf16 bf16x2 __attribute__((ext_vector_type(2)));
typedef __bf16 bf16x8 __attribute__((ext_vector_type(8)));
typedef float f32x2 __attribute__((ext_vector_type(2)));
typedef float f32x4 __attribute__((ext_vector_type(4)));
typedef float f32x16 __attribute__((ext_vector_type(16)));
typedef unsigned u32x2 __attribute__((ext_vector_type(2)));
typedef unsigned u32x4 __attribute__((ext_vector_type(4)));
typedef unsigned short u16;
typedef unsigned long long u64;
#define LAS __attribute__((address_space(3)))

constexpr int L = 2048, D = 640, NM = 128, F = 1728;

#define STAMP_DECL u64 t0_ = 0, tk_ = 0
#define STAMP(i) do { if ((i) == 0) asm volatile("s_memrealtime %0\n\ts_waitcnt lgkmcnt(0)" : "=s"(t0_) :: "memory"); \
    if ((i) == AMPK) asm volatile("s_memrealtime %0\n\ts_waitcnt lgkmcnt(0)" : "=s"(tk_) :: "memory"); } while (0)
#define STAMP_END() do { if (wave == AMPW && tk_ != 0) { const u64 tgt = t0_ + 3000 + 4 * (tk_ - t0_); \
    for (int i_ = 0; i_ < 100000 && __builtin_amdgcn_s_memrealtime() < tgt; ++i_) __builtin_amdgcn_s_sleep(1); } } while (0)
#define ST_ARG
#define ST_PASS
#define STAMPC(i)

#define BAR() asm volatile("s_waitcnt lgkmcnt(0)\n\ts_barrier" ::: "memory")
#define BAR_NOWAIT() asm volatile("s_barrier" ::: "memory")
#define DRAIN() asm volatile("s_waitcnt vmcnt(0)" ::: "memory")
#define SB() __builtin_amdgcn_sched_barrier(0)

__device__ __forceinline__ float bf2f(unsigned x) { return __uint_as_float(x << 16); }
__device__ __forceinline__ f32x2 unpk(unsigned x) {
    f32x2 r; r.x = __uint_as_float(x << 16); r.y = __uint_as_float(x & 0xffff0000u); return r;
}
__device__ __forceinline__ float rbf(float x) { return (float)(__bf16)x; }
__device__ __forceinline__ unsigned f2bf(float x) { __bf16 b = (__bf16)x; return (unsigned)__builtin_bit_cast(u16, b); }
__device__ __forceinline__ unsigned pkbf(f32x2 v) {
    bf16x2 b = __builtin_convertvector(v, bf16x2); return __builtin_bit_cast(unsigned, b);
}
__device__ __forceinline__ f32x2 rbf2(f32x2 v) { return unpk(pkbf(v)); }
__device__ __forceinline__ unsigned pkh(float lo, float hi) {
    f32x2 v = {lo, hi}; f16x2 h = __builtin_convertvector(v, f16x2); return __builtin_bit_cast(unsigned, h);
}
__device__ __forceinline__ unsigned padd(unsigned a, unsigned b) {
    return __builtin_bit_cast(unsigned, __builtin_bit_cast(f16x2, a) + __builtin_bit_cast(f16x2, b));
}
__device__ __forceinline__ float dot2bf(unsigned a, unsigned b, float acc) {
    return __builtin_amdgcn_fdot2_f32_bf16(__builtin_bit_cast(bf16x2, a), __builtin_bit_cast(bf16x2, b), acc, false);
}
__device__ __forceinline__ f16x4 trread(const char* p) {
    return __builtin_bit_cast(f16x4, __builtin_amdgcn_ds_read_tr16_b64_v4i16((LAS s16x4*)p));
}
__device__ __forceinline__ f16x8 cat(f16x4 a, f16x4 b) { return __builtin_shufflevector(a, b, 0, 1, 2, 3, 4, 5, 6, 7); }

template <int CTRL, int RM = 0xf>
__device__ __forceinline__ float dppf(float v) {
    return __builtin_bit_cast(float, __builtin_amdgcn_update_dpp(0, __builtin_bit_cast(int, v), CTRL, RM, 0xf, false));
}
template <int CTRL, int RM = 0xf>
__device__ __forceinline__ unsigned dppu(unsigned v) {
    return (unsigned)__builtin_amdgcn_update_dpp(0, (int)v, CTRL, RM, 0xf, false);
}
__device__ __forceinline__ float row16_sum(float v) {
    v += dppf<0xB1>(v); v += dppf<0x4E>(v); v += dppf<0x141>(v); v += dppf<0x140>(v);
    return v;
}
__device__ __forceinline__ float wave_sum_u(float v) {
    v = row16_sum(v);
    v += dppf<0x142, 0xa>(v);
    v += dppf<0x143, 0xc>(v);
    return __builtin_bit_cast(float, __builtin_amdgcn_readlane(__builtin_bit_cast(int, v), 63));
}
__device__ __forceinline__ unsigned wave_max_u(unsigned v) {
    v = max(v, dppu<0xB1>(v)); v = max(v, dppu<0x4E>(v)); v = max(v, dppu<0x141>(v)); v = max(v, dppu<0x140>(v));
    v = max(v, dppu<0x142, 0xa>(v)); v = max(v, dppu<0x143, 0xc>(v));
    return (unsigned)__builtin_amdgcn_readlane((int)v, 63);
}
__device__ __forceinline__ f32x2 cmul(f32x2 a, f32x2 b) {
    f32x2 o;
    asm("v_pk_mul_f32 %0, %1, %2 op_sel_hi:[0,1]\n\t"
        "v_pk_fma_f32 %0, %1, %2, %0 op_sel:[1,1,0] op_sel_hi:[1,0,1] neg_lo:[0,1,0]"
        : "=&v"(o) : "v"(a), "v"(b));
    return o;
}
__device__ __forceinline__ float exp_acc(float x) {
    const float l2e = 1.44269504088896341f, l2e_lo = 1.92596299e-8f;
    const float t = x * l2e;
    float e = fmaf(x, l2e, -t);
    e = fmaf(x, l2e_lo, e);
    const float r = __builtin_amdgcn_exp2f(t);
    return fmaf(r * e, 0.693147180559945f, r);
}
__device__ __forceinline__ float sigmoid_f(float x) {
    const float e = __builtin_amdgcn_exp2f(fminf(-x * 1.44269504088896341f, 126.f));
    return __builtin_amdgcn_rcpf(1.f + e);
}
__device__ __forceinline__ f32x2 cexp_p(float zl, float wh, float wl, float p) {
    const float mag = __builtin_amdgcn_exp2f(zl * p);
    float t = __builtin_amdgcn_fractf(wh * p);
    t = fmaf(wl, p, t);
    f32x2 o = {__builtin_amdgcn_cosf(t), __builtin_amdgcn_sinf(t)};
    return o * mag;
}
__device__ __forceinline__ void ln_finish(float piv, float s1, float s2, float& mean_b, float& rstd) {
    const float dm = s1 * (1.f / D);
    const float var = fmaxf(fmaf(-dm, dm, s2 * (1.f / D)), 0.f);
    mean_b = rbf(piv + dm);
    rstd = __builtin_amdgcn_rcpf(rbf(__builtin_amdgcn_sqrtf(rbf(rbf(var) + 1e-5f))));
}
__device__ __forceinline__ void row_stats(const f32x2 (&v)[5], float& mb, float& rs) {
    const float piv = __builtin_bit_cast(float, __builtin_amdgcn_readfirstlane(__builtin_bit_cast(int, v[0].x)));
    f32x2 s1 = {0.f, 0.f}, s2 = {0.f, 0.f};
#pragma unroll
    for (int i = 0; i < 5; ++i) { f32x2 d = v[i] - piv; s1 += d; s2 = __builtin_elementwise_fma(d, d, s2); }
    const float t1 = wave_sum_u(s1.x + s1.y), t2 = wave_sum_u(s2.x + s2.y);
    ln_finish(piv, t1, t2, mb, rs);
}

constexpr unsigned WS_EPOCH = 0;
#define FSTR_ 64
constexpr unsigned FSTR = FSTR_;
constexpr unsigned WS_FLAGS = 4096;
constexpr unsigned WS_FLAGS2 = WS_FLAGS + 256 * FSTR;
constexpr unsigned WS_KEYS = WS_FLAGS2 + 256 * FSTR;
constexpr unsigned WS_REC = 131072;
constexpr unsigned WS_Y = 2u << 20;
constexpr unsigned WS_UT = 5u << 20;
constexpr unsigned WS_END = 8u << 20;
constexpr unsigned SENT = 0xffffffffu;
constexpr int SPIN_MAX = 20000;

constexpr int MSTR = 272, TABSZ = NM * MSTR;
constexpr int WSTR = 3200, WSLOT = 4 * WSTR;
constexpr int WG = 3 * WSLOT;
constexpr int KB = 3 * TABSZ, KCS = 1344, T2 = 4 * KCS, T3 = T2 + 79 * 16, KSLOT = T3 + 256;
constexpr int UB = KB + 3 * KSLOT, UOFF = 17 * 80, USLOT = 81 * 80;
constexpr int TRB = UB + 3 * USLOT, RSTR = 1344;
constexpr int MISC = TRB + 8 * RSTR;
constexpr int LDS_BYTES = MISC + 1024;
static_assert(WG + 40960 <= KB && LDS_BYTES <= 163840 && KSLOT % 16 == 0, "lds map");

typedef __amdgpu_buffer_rsrc_t rsrc_t;
__device__ __forceinline__ void st16(rsrc_t r, unsigned off, u32x4 v) { __builtin_amdgcn_raw_buffer_store_b128(v, r, off, 0, 16); }
__device__ __forceinline__ void st8(rsrc_t r, unsigned off, u32x2 v) { __builtin_amdgcn_raw_buffer_store_b64(v, r, off, 0, 16); }
__device__ __forceinline__ void st4(rsrc_t r, unsigned off, unsigned v) { __builtin_amdgcn_raw_buffer_store_b32(v, r, off, 0, 16); }
__device__ __forceinline__ u32x4 ld16(rsrc_t r, unsigned off) { return __builtin_amdgcn_raw_buffer_load_b128(r, off, 0, 16); }
__device__ __forceinline__ u32x2 ld8(rsrc_t r, unsigned off) { return __builtin_amdgcn_raw_buffer_load_b64(r, off, 0, 16); }
__device__ __forceinline__ unsigned ld4(rsrc_t r, unsigned off) { return __builtin_amdgcn_raw_buffer_load_b32(r, off, 0, 16); }
__device__ __forceinline__ bool pending(u32x4 v) { return max(max(v.x, v.y), max(v.z, v.w)) == SENT; }
__device__ __forceinline__ u32x4 poll16(rsrc_t r, unsigned off, u32x4 v) {
    for (int s = 0; s < SPIN_MAX; ++s) {
        const bool pend = pending(v);
        if (!__any(pend)) break;
        __builtin_amdgcn_s_sleep(2);
        asm volatile("" ::: "memory");
        if (pend) v = ld16(r, off);
    }
    return v;
}
template <typename BAD>
__device__ __forceinline__ void sweep4(rsrc_t r, unsigned base, int lane, unsigned (&f)[4], BAD bad) {
    const unsigned off = base + lane * FSTR;
#pragma unroll
    for (int k = 0; k < 4; ++k) f[k] = ld4(r, off + k * 64 * FSTR);
    for (int s = 0; s < SPIN_MAX; ++s) {
        if (!__any(bad(f[0]) || bad(f[1]) || bad(f[2]) || bad(f[3]))) break;
        __builtin_amdgcn_s_sleep(2);
        asm volatile("" ::: "memory");
#pragma unroll
        for (int k = 0; k < 4; ++k) if (bad(f[k])) f[k] = ld4(r, off + k * 64 * FSTR);
    }
}
__device__ __forceinline__ unsigned poll_keys(rsrc_t r, int lane) {
    unsigned f[4];
    sweep4(r, WS_KEYS, lane, f, [](unsigned v) { return v == SENT; });
    return wave_max_u(max(max(f[0], f[1]), max(f[2], f[3])));
}

#define MFMA32(a, b, c) __builtin_amdgcn_mfma_f32_32x32x16_f16(a, b, c, 0, 0, 0)

struct ConvPtr { const char *an, *bn, *a2, *b2, *a3, *b3; };
template <int TILE> struct Steps {
    static constexpr int NN = TILE ? 38 : 35, N2 = TILE ? 24 : 8, N3 = TILE ? 8 : 0, N = NN + N2 + N3;
};
template <int TILE, int S>
__device__ __forceinline__ void conv_ld(const ConvPtr& P, f16x8& A, f16x8& B) {
    using St = Steps<TILE>;
    if constexpr (S < St::NN) {
        constexpr int n = S - 1, nb = n > 33 ? n + 29 : n;
        constexpr int boff = (TILE ? 33 : 17) * 80 + ((nb & 1) ? -((nb + 1) / 2) * 80 + 32 : -(nb / 2) * 80);
        A = cat(trread(P.an + S * 32 + 8), trread(P.an + S * 32));
        B = *(const f16x8*)(P.bn + boff);
    } else if constexpr (S < St::NN + St::N2) {
        constexpr int s = S - St::NN;
        A = cat(trread(P.a2 + s * 128 + 32), trread(P.a2 + s * 128));
        B = *(const f16x8*)(P.b2 + (St::N2 - 1 - s) * 32);
    } else {
        constexpr int s = S - St::NN - St::N2;
        A = cat(trread(P.a3 + s * 256), trread(P.a3 + s * 256 + 64));
        B = cat(*(const f16x4*)(P.b3 + (7 - s) * 32 + 8), *(const f16x4*)(P.b3 + (7 - s) * 32));
    }
}
constexpr int PF = 4;
template <int TILE, int S>
__device__ __forceinline__ void conv_steps(const ConvPtr& P, f16x8 (&A)[PF], f16x8 (&B)[PF], f32x16& acc,
                                           volatile LAS unsigned* cnt, unsigned& seen ST_ARG) {
    using St = Steps<TILE>;
    if constexpr (S < St::N) {
        acc = MFMA32(A[S % PF], B[S % PF], acc);
        if constexpr (S + PF + 4 == St::NN) seen = *cnt;
        if constexpr (S + PF == St::NN) {
            SB();
            for (int s = 0; s < SPIN_MAX && __builtin_amdgcn_readfirstlane(seen) < 8u; ++s) seen = *cnt;
            asm volatile("" ::: "memory");
        }
        if constexpr (S + PF < St::N) conv_ld<TILE, S + PF>(P, A[S % PF], B[S % PF]);
        SB();
        conv_steps<TILE, S + 1>(P, A, B, acc, cnt, seen ST_PASS);
    }
}
__device__ __forceinline__ ConvPtr conv_setup(char* smem, int slot, int tile, int lane) {
    const int c = lane & 31, h = lane >> 5, q = (lane >> 2) & 3;
    const int G = ((lane >> 4) & 1) * 4 + (lane & 3);
    const int tr = 16 * (G & 1) + 4 * (G >> 1);
    const char* wsl = smem + slot * WSLOT;
    const char* ks = smem + KB + slot * KSLOT;
    ConvPtr P;
    P.an = ks + q * KCS + 2 * (tr - 8 * h + 8);
    P.bn = smem + UB + slot * USLOT + UOFF + (c - (tile ? 1 : 17)) * 80 + 16 * h;
    P.a2 = wsl + (q & 1) * WSTR + 2 * (36 + 32 * h + tr - 4 * (q + 1) - 16);
    P.b2 = ks + T2 + (1 + c - h) * 16;
    P.a3 = wsl + (2 + (q & 1)) * WSTR + 2 * (36 + 64 * h + tr - 8 * (q + 1));
    P.b3 = ks + T3 + (c - 2 * h - 29) * 8;
    return P;
}
template <int TILE>
__device__ __forceinline__ void conv_tile(char* smem, const ConvPtr& P, int slot, unsigned yo, rsrc_t wsr ST_ARG) {
    f16x8 A[PF], B[PF];
    conv_ld<TILE, 0>(P, A[0], B[0]); conv_ld<TILE, 1>(P, A[1], B[1]);
    conv_ld<TILE, 2>(P, A[2], B[2]); conv_ld<TILE, 3>(P, A[3], B[3]);
    const float sc = *(const float*)(smem + MISC + 96 + slot * 4);
    f32x16 acc;
#pragma unroll
    for (int i = 0; i < 16; ++i) acc[i] = 0.f;
    SB();
    unsigned seen = 0;
    conv_steps<TILE, 0>(P, A, B, acc, (volatile LAS unsigned*)(smem + MISC + 48), seen ST_PASS);
#pragma unroll
    for (int g = 0; g < 2; ++g) {
        u32x4 o;
#pragma unroll
        for (int e = 0; e < 4; ++e) o[e] = pkbf((f32x2){acc[8 * g + 2 * e], acc[8 * g + 2 * e + 1]} * sc);
        st16(wsr, yo + g * 1024, o);
    }
}

template <bool FULL>
__device__ __forceinline__ void conv_windows(char* smem, int slot, int i, int lane) {
    const char* op = smem + UB + slot * USLOT + UOFF + (lane >> 2) * 80 + (lane & 3) * 16 + i * 1280;
    const u32x4 own = *(const u32x4*)op;
    const u32x4 pv = *(const u32x4*)(op - ((lane & 3) ? 16 : 32));
    unsigned w[8] = {pv.x, pv.y, pv.z, pv.w, own.x, own.y, own.z, own.w};
    unsigned a[7];
#pragma unroll
    for (int k = 0; k < 7; ++k) a[k] = __builtin_amdgcn_alignbit(w[k + 1], w[k], 16);
    u32x4 w3, w5, w7, w9;
#pragma unroll
    for (int k = 0; k < 4; ++k) {
        w3[k] = padd(w[2 + k], padd(a[1 + k], a[2 + k]));
        w5[k] = padd(w3[k], padd(w[1 + k], w[3 + k]));
        if (FULL) {
            w7[k] = padd(w5[k], padd(a[k], a[3 + k]));
            w9[k] = padd(w7[k], padd(w[k], w[4 + k]));
        }
    }
    char* wp = smem + slot * WSLOT + 64 + lane * 16 + i * 1024;
    *(u32x4*)(wp) = w3;
    *(u32x4*)(wp + WSTR) = w5;
    if (FULL) {
        *(u32x4*)(wp + 2 * WSTR) = w7;
        *(u32x4*)(wp + 3 * WSTR) = w9;
    }
}

#define SLP_FLAG 6
#define SLP_F2 16
#define SLP_CLR 12
#define SLP_KEYS 16
#define SLP_W 12
__device__ __forceinline__ void lds_add1(char* q) {
    __hip_atomic_fetch_add((LAS unsigned*)q, 1u, __ATOMIC_RELAXED, __HIP_MEMORY_SCOPE_WORKGROUP);
}

__device__ __forceinline__ void lds_wait(char* q, unsigned n) {
    for (int s = 0; s < SPIN_MAX; ++s) {
        if (__builtin_amdgcn_readfirstlane(*(volatile LAS unsigned*)q) >= n) break;
        __builtin_amdgcn_s_sleep(1);
    }
    asm volatile("" ::: "memory");
}
#define LGKM0() asm volatile("s_waitcnt lgkmcnt(0)" ::: "memory")
#define RB2(v) rbf2(v)

extern "C" __global__ void __launch_bounds__(1024, 4)
fused_kernel(const u16* __restrict__ x, const u16* __restrict__ logA, const u16* __restrict__ Aim,
             const u16* __restrict__ Cre, const u16* __restrict__ Cim, const u16* __restrict__ logdt,
             const u16* __restrict__ dskip, const u16* __restrict__ wr, const u16* __restrict__ wg,
             const u16* __restrict__ wu, const u16* __restrict__ wd, u16* out, char* ws) {
    extern __shared__ __attribute__((aligned(16))) char smem[];
    asm volatile("" :: "s"(x), "s"(logA), "s"(Aim), "s"(Cre), "s"(Cim), "s"(logdt), "s"(dskip), "s"(wr), "s"(wg), "s"(wu),
                 "s"(wd), "s"(out), "s"(ws));
    const int tid = threadIdx.x, lane = tid & 63;
    const int wave = __builtin_amdgcn_readfirstlane(tid >> 6);
    const int p = (blockIdx.x & 7) * 32 + (blockIdx.x >> 3);
    const int isB = p & 1;
    const int d0 = 5 * (p >> 1) + 2 * isB;
    const int fs = (p >> 5) * 14 + (p & 31);
    const bool ffn = (p & 31) < 14 && fs < 108;
    const rsrc_t wsr = __builtin_amdgcn_make_buffer_rsrc(ws, 0, WS_END, 0x00020000);
    const u32x4 s4 = {SENT, SENT, SENT, SENT};
    STAMP_DECL; STAMP(0);

    auto ld5 = [&](const u16* q_, unsigned (&o)[5]) {
        u32x2 a = *(const u32x2*)(q_ + lane * 4), b = *(const u32x2*)(q_ + 256 + lane * 4);
        o[0] = a.x; o[1] = a.y; o[2] = b.x; o[3] = b.y; o[4] = *(const unsigned*)(q_ + 512 + lane * 2);
    };
    auto chof = [&](int s) { return isB ? d0 + (s == 2 ? 0 : s + 1) : d0 + s; };
    const int row = p * 8 + (wave & 7);
    const int sub = wave & 3;
    unsigned xr[5];
    unsigned tag = 0, cpf = 0;
    float mb1 = 0.f, rs1 = 0.f;
    if (wave < 8) {
        ld5(x + (size_t)row * D, xr);
        tag = ld4(wsr, WS_EPOCH);
        {
            u64 pc; unsigned off, len;
            asm volatile("s_getpc_b64 %0\n.Lfk_pc:" : "=s"(pc));
            asm volatile("s_mov_b32 %0, .Lfk_pc-fused_kernel\n\ts_mov_b32 %1, .Lfk_end-fused_kernel" : "=s"(off), "=s"(len));
            const rsrc_t cr = __builtin_amdgcn_make_buffer_rsrc((void*)(pc - off), 0, len, 0x00020000);
            cpf = __builtin_amdgcn_raw_buffer_load_b32(cr, tid * 128, 0, 0);
        }
        if (lane < 4) *(u32x4*)(smem + MISC + 32 + lane * 16) = (u32x4){0u, 0u, 0u, 0u};
        char* tp = smem + TRB + wave * RSTR + lane * 4;
        const char* tp2 = smem + TRB + ((lane >> 2) & 3) * RSTR + ((lane >> 4) * 16 + (lane & 3) * 4) * 2 + wave * 128;
        const unsigned uso = WS_UT + (p * D + tid) * 16;
        BAR();
        __builtin_amdgcn_s_setprio(3);
        f32x2 v[5];
#pragma unroll
        for (int i = 0; i < 5; ++i) v[i] = unpk(xr[i]);
        STAMP(1);
        row_stats(v, mb1, rs1);
        unsigned uh[5];
#pragma unroll
        for (int i = 0; i < 5; ++i) {
            const f32x2 u = RB2(RB2(v[i] - mb1) * rs1);
            uh[i] = pkh(u.x, u.y);
        }
        *(u32x2*)(tp + lane * 4) = (u32x2){uh[0], uh[1]};
        *(u32x2*)(tp + 512 + lane * 4) = (u32x2){uh[2], uh[3]};
        *(unsigned*)(tp + 1024) = uh[4];
        tag = __builtin_amdgcn_readfirstlane(tag) + 1u;
        asm volatile("s_waitcnt lgkmcnt(0)" : "+s"(tag) :: "memory");
        if (lane == 0) lds_add1(smem + MISC + 52);
        lds_wait(smem + MISC + 52, 9u);
        STAMP(2);
#pragma unroll
        for (int i = 0; i < 2; ++i) {
            if (i < 1 || wave < 2) {
                const f16x8 o = cat(trread(tp2 + i * 1024), trread(tp2 + i * 1024 + 4 * RSTR));
                st16(wsr, uso + i * 8192, __builtin_bit_cast(u32x4, o));
            }
        }
        __builtin_amdgcn_s_setprio(0);
        STAMP(3);
    } else {
        const int slot = wave < 12 ? 1 + (sub >> 1) : 0;
        const bool doA = wave < 14, doB = wave < 12 || wave >= 14;
        const int dch = chof(slot);
        if (wave == 15) {
#pragma unroll
            for (int i = 0; i < 10; ++i) st16(wsr, WS_UT + (p * D + i * 64 + lane) * 16, s4);
            tag = ld4(wsr, WS_EPOCH);
        }
        const unsigned ldt_ = ((const unsigned*)logdt)[dch >> 1];
        const unsigned la_ = ((const unsigned*)(logA + dch * NM))[lane];
        const unsigned ai_ = ((const unsigned*)(Aim + dch * NM))[lane];
        const unsigned c0 = ((const unsigned*)(Cre + dch * NM))[lane];
        const unsigned c1 = ((const unsigned*)(Cim + dch * NM))[lane];
        BAR();
        {
            const u32x4 z = {0u, 0u, 0u, 0u};
            const int zi = tid - 512;
#pragma unroll
            for (int i = 0; i < 3; ++i) if (i < 2 || zi < 3 * KSLOT / 16 - 1024) *(u32x4*)(smem + KB + (i * 512 + zi) * 16) = z;
            if (zi < 384 && (zi & 127) < 85) *(u32x4*)(smem + UB + (zi >> 7) * USLOT + (zi & 127) * 16) = z;
            LGKM0();
            if (lane == 0) lds_add1(smem + MISC + 56);
        }
        if (wave == 15) {
            DRAIN();
            if (lane == 0) { st4(wsr, WS_FLAGS + p * FSTR, tag + 1u); lds_add1(smem + MISC + 52); }
            __builtin_amdgcn_s_setprio(2);
            STAMP(3);
        }
        const unsigned sh = (wave & 1) ? 0u : 16u;
        auto pick = [&](unsigned w) { return __uint_as_float((w << sh) & 0xffff0000u); };
        const float dt = rbf(exp_acc(__uint_as_float((dch & 1) ? ldt_ & 0xffff0000u : ldt_ << 16)));
        const float zr = -rbf(exp_acc(pick(la_))) * dt;
        const float zi = pick(ai_) * dt;
        const float zl = zr * 1.44269504088896341f;
        const float wh = zi * 0.15915494309189535f;
        const float wl = fmaf(zi, 6.4206383e-09f, fmaf(zi, 0.15915494309189535f, -wh));
        STAMP(1);
        const f32x2 e8 = cexp_p(zl, wh, wl, 8.f);
        const f32x2 e128 = cexp_p(zl, wh, wl, 128.f);
        const f32x2 e256 = cmul(e128, e128), e384 = cmul(e128, e256), e512 = cmul(e256, e256);
        char* tb = smem + slot * TABSZ + ((wave & 1) * 64 + lane) * MSTR;
        auto put8 = [&](int grp, const f32x2 (&e)[8]) {
            u32x4 re = {pkh(e[0].x, e[1].x), pkh(e[2].x, e[3].x), pkh(e[4].x, e[5].x), pkh(e[6].x, e[7].x)};
            u32x4 im = {pkh(e[0].y, e[1].y), pkh(e[2].y, e[3].y), pkh(e[4].y, e[5].y), pkh(e[6].y, e[7].y)};
            *(u32x4*)(tb + grp * 16) = re;
            *(u32x4*)(tb + 128 + grp * 16) = im;
        };
        if (doA) {
            f32x2 e[8], g[8];
            e[0] = (f32x2){1.f, 0.f};
            e[1] = cexp_p(zl, wh, wl, 1.f);
            e[2] = cmul(e[1], e[1]); e[4] = cmul(e[2], e[2]);
            e[3] = cmul(e[1], e[2]); e[5] = cmul(e[1], e[4]); e[6] = cmul(e[2], e[4]); e[7] = cmul(e[3], e[4]);
            put8(0, e);
            g[0] = e128;
#pragma unroll
            for (int r = 1; r < 8; ++r) g[r] = cmul(e[r], e128);
            put8(1, g);
            g[0] = cmul(e[2], e256); g[1] = cmul(e[4], e256); g[2] = cmul(e[6], e256); g[3] = cmul(e8, e256);
            g[4] = cmul(e[2], e384); g[5] = cmul(e[4], e384); g[6] = cmul(e[6], e384); g[7] = cmul(e8, e384);
            put8(2, g);
            g[0] = cmul(e512, e[4]); g[1] = cmul(e512, e8);
            g[2] = cmul(g[0], e128); g[3] = cmul(g[1], e128);
            g[4] = cmul(g[0], e256); g[5] = cmul(g[1], e256);
            g[6] = cmul(g[0], e384); g[7] = cmul(g[1], e384);
            put8(3, g);
        }
        if (doB) {
            const unsigned m0 = c0 & 0x7fff7fffu, m1 = c1 & 0x7fff7fffu;
            const unsigned mx = wave_max_u(max(max(m0 >> 16, m0 & 0xffffu), max(m1 >> 16, m1 & 0xffffu)));
            const int ex = min(max((int)(mx >> 7), 8), 246);
            const float up = __uint_as_float((unsigned)(260 - ex) << 23);
            if (lane == 0) *(float*)(smem + MISC + 96 + slot * 4) = 2.f * dt * __uint_as_float((unsigned)(ex - 6) << 23);
            const f32x2 c8 = {e8.x, -e8.y};
            const f32x2 c16 = cmul(c8, c8), c32 = cmul(c16, c16), c64 = cmul(c32, c32);
            const f32x2 e1024 = cmul(e512, e512);
            f32x2 b[8], g[8];
            g[0] = cmul(e1024, e8); g[1] = cmul(g[0], e128); g[2] = cmul(g[0], e256); g[3] = cmul(g[0], e384);
#pragma unroll
            for (int s = 0; s < 4; ++s) g[4 + s] = cmul(g[s], e512);
            put8(4, g);
            *(u32x2*)(tb + 80) = (u32x2){pkh(e256.x, e512.x), pkh(e1024.x, 0.f)};
            *(u32x2*)(tb + 128 + 80) = (u32x2){pkh(e256.y, e512.y), pkh(e1024.y, 0.f)};
            b[0] = (f32x2){pick(c0) * up, -pick(c1) * up};
            b[1] = cmul(b[0], c8); b[2] = cmul(b[0], c16); b[3] = cmul(b[1], c16);
#pragma unroll
            for (int j = 0; j < 4; ++j) b[4 + j] = cmul(b[j], c32);
            put8(6, b);
#pragma unroll
            for (int j = 0; j < 8; ++j) g[j] = cmul(b[j], c64);
            put8(7, g);
        }
        LGKM0();
        if (lane == 0) lds_add1(smem + MISC + 64 + slot * 4);
        __builtin_amdgcn_s_setprio(0);
        STAMP(4);
    }
    if (wave >= 8 || wave == 3) {
        const int ts = wave == 3 ? 2 : (wave == 15 ? 1 : (wave < 12 ? 1 + (sub >> 1) : 0));
        const int tt = sub == 3 ? (wave == 11 ? 1 : 2) : (wave < 12 ? (sub & 1) : sub);
        char* ks = smem + KB + ts * KSLOT;
        const int qq = lane >> 4, q = (lane >> 2) & 3, pp = lane & 3, j = lane & 15;
        const char* ap = smem + ts * TABSZ + (4 * qq + 2 * (q >> 1)) * MSTR + (q & 1) * 128 + pp * 8;
        const char* bp = ap + (tt == 2 ? 64 : 96);
        ap += tt == 2 ? 96 : tt * 32;
        lds_wait(smem + MISC + 56, 8u);
        lds_wait(smem + MISC + 64 + ts * 4, ts ? 2u : 4u);
        STAMP(5);
        f16x8 av[8], bv[8];
#pragma unroll
        for (int k = 0; k < 8; ++k) {
            av[k] = cat(trread(ap + k * 16 * MSTR), trread(ap + k * 16 * MSTR + MSTR));
            bv[k] = cat(trread(bp + k * 16 * MSTR), trread(bp + k * 16 * MSTR + MSTR));
        }
        SB();
        f32x4 acc = {0.f, 0.f, 0.f, 0.f}, acc1 = {0.f, 0.f, 0.f, 0.f};
#pragma unroll
        for (int k = 0; k < 4; ++k) {
            acc = __builtin_amdgcn_mfma_f32_16x16x32_f16(av[2 * k], bv[2 * k], acc, 0, 0, 0);
            acc1 = __builtin_amdgcn_mfma_f32_16x16x32_f16(av[2 * k + 1], bv[2 * k + 1], acc1, 0, 0, 0);
        }
        acc += acc1;
        const unsigned p01 = pkh(acc[0], acc[1]), p23 = pkh(acc[2], acc[3]), p12 = pkh(acc[1], acc[2]);
        if (tt == 0) {
            char* o = ks + 2 * (28 + 4 * (qq & 1) + 128 * (qq >> 1) + 8 * j);
            *(u32x2*)o = (u32x2){p01, p23};
            *(unsigned*)(o + 2 * KCS + 4) = p01; *(unsigned*)(o + 2 * KCS + 8) = p23;
            *(u16*)(o + KCS + 2) = (u16)p01; *(unsigned*)(o + KCS + 4) = p12; *(u16*)(o + KCS + 8) = (u16)(p23 >> 16);
            *(u16*)(o + 3 * KCS + 6) = (u16)p01; *(unsigned*)(o + 3 * KCS + 8) = p12; *(u16*)(o + 3 * KCS + 12) = (u16)(p23 >> 16);
        } else if (tt == 1) {
            if (qq < 2) {
                const unsigned p11 = pkh(acc[1], acc[1]), p33 = pkh(acc[3], acc[3]);
                char* o = ks + 2 * (28 + 258 + 128 * qq + 8 * j);
                unsigned* o0 = (unsigned*)o;
                o0[0] = p01; o0[1] = p11; o0[2] = p23; o0[3] = p33;
                *(u32x4*)(o + 2 * KCS + 4) = (u32x4){p01, p11, p23, p33};
                *(u16*)(o + KCS + 2) = (u16)p01; *(u32x2*)(o + KCS + 4) = (u32x2){p11, p12};
                *(unsigned*)(o + KCS + 12) = p33; *(u16*)(o + KCS + 16) = (u16)p33;
                *(u16*)(o + 3 * KCS + 6) = (u16)p01; *(unsigned*)(o + 3 * KCS + 8) = p11;
                *(u32x2*)(o + 3 * KCS + 12) = (u32x2){p12, p33}; *(u16*)(o + 3 * KCS + 20) = (u16)p33;
            } else {
                unsigned* t2 = (unsigned*)(ks + T2 + 31 * 16) + 32 * (qq - 2) + j;
                t2[0] = p01; t2[16] = p23;
            }
        } else {
            if (j < 8) *(u32x2*)(ks + T3 + (16 * j + 4 * qq) * 2) = (u32x2){p01, p23};
            const unsigned s256 = __builtin_amdgcn_readlane((int)p01, 8), s512 = __builtin_amdgcn_readlane((int)p01, 9),
                           s1024 = __builtin_amdgcn_readlane((int)p01, 10);
            const int w = lane >> 2, cq = lane & 3;
            const int lg = w < 2 ? 256 + w : (w == 2 ? 514 : 560 + w);
            const unsigned sv = w < 2 ? s256 : (w == 2 ? s512 : s1024);
            if (w < 5) *(u16*)(ks + cq * KCS + 2 * (lg + cq + 28)) = (u16)sv;
        }
        if (wave >= 12) {
            const int xi = tid - 768;
            const int xg = p >> 5, li = (p & 31) * 160 + xi;
            const u16* src;
            if (xi < 160) src = ((li & 4) ? wu : wg) + (size_t)(li >> 3) * F + min(((7 * xg) >> 1) + (li & 3), 26) * 64;
            else src = wd + (size_t)min(xg * 2240 + (p & 31) * 70 + min(xi - 160, 69), F * 10 - 1) * 64;
            cpf = *(const unsigned*)src;
        }
        STAMP(6);
    } else if (wave < 3) {
        __builtin_amdgcn_s_sleep(SLP_CLR);
        if (wave) {
#pragma unroll
            for (int i = 0; i < 5; ++i) {
                const int ii = (wave - 1) * 5 + i;
                const int k = isB ? (ii < 2 ? ii : ii + 2) : (ii < 8 ? ii : ii + 2);
                st16(wsr, WS_Y + (d0 * 256 + k * 64 + lane) * 16, s4);
            }
        } else {
#pragma unroll
            for (int i = 0; i < 5; ++i) if (i < 3 || ffn) st16(wsr, WS_REC + p * 5120 + (i * 64 + lane) * 16, s4);
            if (lane == 0) st4(wsr, WS_KEYS + p * FSTR, SENT);
        }
        DRAIN();
        STAMP(6);
    } else {
        const unsigned fo = WS_FLAGS + (tid - 256) * FSTR;
        unsigned uo[3], lo[3];
#pragma unroll
        for (int i = 0; i < 3; ++i) {
            const int idx = i * 64 + lane, g0 = idx / 3, uc = idx - 3 * g0, ug = (wave - 4) * 64 + g0;
            const int us = isB ? (uc == 0 ? 2 : uc - 1) : uc;
            uo[i] = WS_UT + (ug * D + d0 + uc) * 16;
            lo[i] = UB + us * USLOT + UOFF + (ug >> 2) * 80 + (ug & 3) * 16;
        }
        unsigned f = ld4(wsr, fo);
        for (int s = 0; s < SPIN_MAX; ++s) {
            if (!__any(f != tag)) break;
            __builtin_amdgcn_s_sleep(2);
            asm volatile("" ::: "memory");
            if (f != tag) f = ld4(wsr, fo);
        }
        asm volatile("" ::: "memory");
        STAMP(4);
        u32x4 uv[3];
#pragma unroll
        for (int i = 0; i < 3; ++i) uv[i] = ld16(wsr, uo[i]);
#pragma unroll
        for (int i = 0; i < 3; ++i) {
            uv[i] = poll16(wsr, uo[i], uv[i]);
            *(u32x4*)(smem + lo[i]) = uv[i];
        }
        STAMP(6);
    }
    const int cslot = wave == 11 ? 0 : (wave == 15 ? 1 : wave - 8);
    const int ctile = (sub == 3 || (wave == 10 && isB)) ? 0 : 1;
    const ConvPtr CP = conv_setup(smem, cslot, ctile, lane);
    const unsigned cyo = WS_Y + (chof(cslot) * 256 + ctile * 128 + lane) * 16;
    STAMP(7);
    BAR();
    STAMP(8);
    unsigned wdp[16], er[5], wv[5];
    if (wave < 8) {
        ld5(dskip, er);
        ld5(wr, wv);
        if (tid == 448) {
            st4(wsr, WS_FLAGS2 + p * FSTR, tag);
            if (p == 0) st4(wsr, WS_EPOCH, tag);
        }
        if (wave < 6) conv_windows<true>(smem, wave >> 1, wave & 1, lane);
        else {
            conv_windows<false>(smem, wave - 6, 2, lane);
            if (wave == 6) conv_windows<false>(smem, 2, 2, lane);
            else if (lane < 48) *(u32x4*)(smem + (lane >> 2) * WSTR + (lane & 3) * 16) = (u32x4){0u, 0u, 0u, 0u};
        }
        LGKM0();
        if (lane == 0) lds_add1(smem + MISC + 48);
        STAMP(9);
#pragma unroll
        for (int i = 0; i < 5; ++i) er[i] = pkbf(RB2(RB2(unpk(xr[i]) - mb1) * rs1) * unpk(er[i]));
        if (ffn && wave < 5) {
            const unsigned* wp = (const unsigned*)(wd + (size_t)fs * 16 * D) + tid;
#pragma unroll
            for (int i = 0; i < 16; ++i) wdp[i] = wp[i * (D / 2)];
        }
        if (wave == 7) {
            __builtin_amdgcn_s_sleep(SLP_F2);
            unsigned f[4];
            sweep4(wsr, WS_FLAGS2, lane, f, [&](unsigned v) { return v != tag; });
        }
        STAMP(5);
    } else if (wave < 12 || wave == 15) {
        if (wave < 12) STAMPC(2);
        if (ctile) conv_tile<1>(smem, CP, cslot, cyo, wsr ST_PASS);
        else conv_tile<0>(smem, CP, cslot, cyo, wsr ST_PASS);
        if (wave < 12) STAMPC(3);
        STAMP(9);
        __builtin_amdgcn_s_setprio(0);
        DRAIN();
    } else if (ffn) {
        u32x4 wq[14];
        const int xi = tid - 768;
#pragma unroll
        for (int i = 0; i < 14; ++i) {
            const int ci = min(i * 192 + xi, 2559), r_ = ci >> 1;
            const u16* wsrc = r_ < D ? wg + (size_t)r_ * F : wu + (size_t)(r_ - D) * F;
            if (i < 13 || wave == 12) wq[i] = *(const u32x4*)(wsrc + fs * 16 + (ci & 1) * 8);
        }
        SB();
#pragma unroll
        for (int i = 0; i < 14; ++i) if (i < 13 || wave == 12) *(u32x4*)(smem + WG + (i * 192 + xi) * 16) = wq[i];
        STAMP(9);
    }
    asm volatile("" :: "v"(cpf));
    BAR();
    STAMP(10);
    if (wave < 10) {
        const unsigned yoff = WS_Y + (tid * 256 + (p >> 7) * 128 + (p & 1) * 64 + ((p >> 1) & 1) * 32 + ((p >> 2) & 31)) * 16;
        const u32x4 yv = poll16(wsr, yoff, ld16(wsr, yoff));
        u16* yp = (u16*)(smem + TRB) + tid;
#pragma unroll
        for (int i = 0; i < 4; ++i) {
            yp[(2 * i) * D] = (u16)yv[i];
            yp[(2 * i + 1) * D] = (u16)(yv[i] >> 16);
        }
    }
    STAMP(11);
    BAR();
    unsigned op[5];
    bool plain_out = false;
    if (wave < 8) {
        unsigned yr[5], hp[5], zp[5];
        ld5((const u16*)(smem + TRB) + wave * D, yr);
        f32x2 hv[5];
#pragma unroll
        for (int i = 0; i < 5; ++i) {
            hp[i] = pkbf(unpk(xr[i]) + RB2(unpk(yr[i]) + unpk(er[i])));
            hv[i] = unpk(hp[i]);
        }
        float mb, rs;
        row_stats(hv, mb, rs);
        float ra = 0.f;
#pragma unroll
        for (int i = 0; i < 5; ++i) {
            zp[i] = pkbf(RB2(hv[i] - mb) * rs);
            ra = dot2bf(zp[i], wv[i], ra);
        }
        const float r = wave_sum_u(ra);
        unsigned rb = f2bf(r);
        rb = (rb & 0x8000u) ? (~rb & 0xffffu) : (rb | 0x8000u);
        const unsigned key = (rb << 16) | (unsigned)(2047 - row);
        if (lane == 0) *(unsigned*)(smem + MISC + wave * 4) = key;
        BAR();
        STAMP(12);
        const u32x4 k0 = *(const u32x4*)(smem + MISC), k1 = *(const u32x4*)(smem + MISC + 16);
        const unsigned best = max(max(max(k0.x, k0.y), max(k0.z, k0.w)), max(max(k1.x, k1.y), max(k1.z, k1.w)));
        plain_out = __builtin_amdgcn_readfirstlane(best) != key;
        if (!plain_out) {
            const unsigned rec = WS_REC + p * 5120;
            if (lane == 0) st4(wsr, WS_KEYS + p * FSTR, key);
            st8(wsr, rec + lane * 8, (u32x2){zp[0], zp[1]});
            st8(wsr, rec + 512 + lane * 8, (u32x2){zp[2], zp[3]});
            st4(wsr, rec + 1024 + lane * 4, zp[4]);
        }
#pragma unroll
        for (int i = 0; i < 5; ++i) op[i] = pkbf(hv[i] + unpk(zp[i]));
        if (!plain_out) {
            const unsigned rec = WS_REC + p * 5120;
            const rsrc_t orr = __builtin_amdgcn_make_buffer_rsrc(out + (size_t)row * D, 0, D * 2, 0x00020000);
            st8(orr, lane * 8, (u32x2){op[0], op[1]});
            st8(orr, 512 + lane * 8, (u32x2){op[2], op[3]});
            st4(orr, 1024 + lane * 4, op[4]);
            DRAIN();
            st8(wsr, rec + 1280 + lane * 8, (u32x2){hp[0], hp[1]});
            st8(wsr, rec + 1280 + 512 + lane * 8, (u32x2){hp[2], hp[3]});
            st4(wsr, rec + 1280 + 1024 + lane * 4, hp[4]);
        }
    } else {
        BAR();
        STAMP(12);
    }
    if (ffn) {
        f16x8 bw[4];
        const int mat = wave >= 5, kgp = wave - 5 * mat;
        if (wave < 10) {
            const char* bp = smem + WG + mat * 20480 + (kgp * 128 + (lane >> 4) * 8 + ((lane >> 2) & 3)) * 32 + (lane & 3) * 8;
#pragma unroll
            for (int k = 0; k < 4; ++k) bw[k] = cat(trread(bp + k * 1024), trread(bp + k * 1024 + 128));
        } else if (wave == 15) {
            __builtin_amdgcn_s_sleep(SLP_KEYS);
            const unsigned bk = poll_keys(wsr, lane);
            if (lane == 0) *(unsigned*)(smem + MISC + 32) = bk;
        }
        unsigned wq[16];
        if (wave < 5) {
#pragma unroll
            for (int i = 0; i < 8; ++i) {
                wq[2 * i] = __builtin_amdgcn_perm(wdp[2 * i + 1], wdp[2 * i], 0x05040100u);
                wq[2 * i + 1] = __builtin_amdgcn_perm(wdp[2 * i + 1], wdp[2 * i], 0x07060302u);
            }
        }
        BAR();
        STAMP(13);
        if (wave < 10) {
            const unsigned bk = *(const unsigned*)(smem + MISC + 32);
            const unsigned xo = WS_REC + ((2047u - (bk & 2047u)) >> 3) * 5120 + (kgp * 128 + (lane >> 4) * 8) * 2;
            u32x4 xa[4];
#pragma unroll
            for (int k = 0; k < 4; ++k) xa[k] = ld16(wsr, xo + k * 64);
#pragma unroll
            for (int k = 0; k < 4; ++k) xa[k] = poll16(wsr, xo + k * 64, xa[k]);
            f32x4 acc = {0.f, 0.f, 0.f, 0.f};
#pragma unroll
            for (int k = 0; k < 4; ++k)
                acc = __builtin_amdgcn_mfma_f32_16x16x32_bf16(__builtin_bit_cast(bf16x8, xa[k]), __builtin_bit_cast(bf16x8, bw[k]), acc, 0, 0, 0);
            if (lane < 16) *(float*)(smem + MISC + 128 + (wave * 16 + lane) * 4) = acc[0];
        }
        BAR();
        STAMP(14);
        if (wave < 5) {
            const float* pr = (const float*)(smem + MISC + 128) + (lane & 15);
            const float g = rbf(pr[0] + pr[16] + pr[32] + pr[48] + pr[64]);
            const float u = rbf(pr[80] + pr[96] + pr[112] + pr[128] + pr[144]);
            const unsigned a = f2bf(rbf(g * sigmoid_f(g)) * u);
            const unsigned ap = a | (dppu<0xB1>(a) << 16);
            float y0 = 0.f, y1 = 0.f;
#pragma unroll
            for (int i = 0; i < 8; ++i) {
                const unsigned s = (unsigned)__builtin_amdgcn_readlane((int)ap, 2 * i);
                y0 = dot2bf(wq[2 * i], s, y0);
                y1 = dot2bf(wq[2 * i + 1], s, y1);
            }
            st8(wsr, WS_REC + p * 5120 + 2560 + tid * 8, (u32x2){__float_as_uint(y0), __float_as_uint(y1)});
        }
        STAMP(15);
    } else if (wave < 8) {
        for (int i = 0; i <= wave; ++i) __builtin_amdgcn_s_sleep(6);
    } else if ((p & 31) >= 16 && (p & 31) < 26 && wave >= 14) {
        const int cq = 2 * ((p >> 5) * 10 + (p & 31) - 16) + (wave - 14);
        __builtin_amdgcn_s_sleep(40);
        const unsigned bk = poll_keys(wsr, lane);
        const int wrow = 2047 - (int)(bk & 2047u);
        const unsigned zo = WS_REC + (wrow >> 3) * 5120 + cq * 8 + (lane & 1) * 1280;
        u32x2 zh = ld8(wsr, zo);
        const int f0 = min(lane, 53), f1 = f0 + 54;
        const unsigned po0 = WS_REC + ((f0 / 14) * 32 + f0 % 14) * 5120 + 2560 + cq * 16;
        const unsigned po1 = WS_REC + ((f1 / 14) * 32 + f1 % 14) * 5120 + 2560 + cq * 16;
        for (int s = 0; s < SPIN_MAX; ++s) {
            const bool pend = max(zh.x, zh.y) == SENT;
            if (!__any(pend)) break;
            __builtin_amdgcn_s_sleep(2);
            asm volatile("" ::: "memory");
            if (pend) zh = ld8(wsr, zo);
        }
        unsigned rb = bk >> 16;
        rb = (rb & 0x8000u) ? (rb & 0x7fffu) : (~rb & 0xffffu);
        const float rsel = rbf(sigmoid_f(bf2f(rb)));
        STAMP(13);
        u32x4 v0 = ld16(wsr, po0), v1 = ld16(wsr, po1);
        v0 = poll16(wsr, po0, v0);
        v1 = poll16(wsr, po1, v1);
        STAMP(14);
        f32x4 sv = __builtin_bit_cast(f32x4, v0) + __builtin_bit_cast(f32x4, v1);
        if (lane >= 54) sv = (f32x4){0.f, 0.f, 0.f, 0.f};
        float sm[4];
#pragma unroll
        for (int e = 0; e < 4; ++e) sm[e] = wave_sum_u(sv[e]);
        const unsigned z01 = __builtin_amdgcn_readlane((int)zh.x, 0), z23 = __builtin_amdgcn_readlane((int)zh.y, 0);
        const unsigned h01 = __builtin_amdgcn_readlane((int)zh.x, 1), h23 = __builtin_amdgcn_readlane((int)zh.y, 1);
        const f32x2 ya = rbf2(rbf2((f32x2){sm[0], sm[1]}) * rsel), yb = rbf2(rbf2((f32x2){sm[2], sm[3]}) * rsel);
        u32x2 o;
        o.x = pkbf(unpk(h01) + rbf2(unpk(z01) + ya));
        o.y = pkbf(unpk(h23) + rbf2(unpk(z23) + yb));
        if (lane == 0) *(u32x2*)(out + (size_t)wrow * D + cq * 4) = o;
        STAMP(11);
    }
    if (plain_out) {
        u16* q_ = out + (size_t)row * D;
        *(u32x2*)(q_ + lane * 4) = (u32x2){op[0], op[1]};
        *(u32x2*)(q_ + 256 + lane * 4) = (u32x2){op[2], op[3]};
        *(unsigned*)(q_ + 512 + lane * 2) = op[4];
    }
    STAMP_END();
    asm volatile(".Lfk_end:");
}

extern "C" void kernel_launch(void* const* d_in, const int* in_sizes, int n_in, void* d_out, int out_size,
                              void* d_ws, size_t ws_size, hipStream_t stream) {
    (void)hipFuncSetAttribute((const void*)fused_kernel, hipFuncAttributeMaxDynamicSharedMemorySize, LDS_BYTES);
    fused_kernel<<<256, 1024, LDS_BYTES, stream>>>(
        (const u16*)d_in[0], (const u16*)d_in[1], (const u16*)d_in[2], (const u16*)d_in[3], (const u16*)d_in[4],
        (const u16*)d_in[5], (const u16*)d_in[6], (const u16*)d_in[8], (const u16*)d_in[9], (const u16*)d_in[10],
        (const u16*)d_in[11], (u16*)d_out, (char*)d_ws);
}
```

```cpp
#include <hip/hip_runtime.h>
#include <hip/hip_bf16.h>
#include <stdint.h>

typedef _Float16 f16;
typedef f16 f16x2 __attribute__((ext_vector_type(2)));
typedef f16 f16x4 __attribute__((ext_vector_type(4)));
typedef f16 f16x8 __attribute__((ext_vector_type(8)));
typedef short s16x4 __attribute__((ext_vector_type(4)));
typedef __bf16 bf16x2 __attribute__((ext_vector_type(2)));
typedef __bf16 bf16x8 __attribute__((ext_vector_type(8)));
typedef float f32x2 __attribute__((ext_vector_type(2)));
typedef float f32x4 __attribute__((ext_vector_type(4)));
typedef float f32x16 __attribute__((ext_vector_type(16)));
typedef unsigned u32x2 __attribute__((ext_vector_type(2)));
typedef unsigned u32x4 __attribute__((ext_vector_type(4)));
typedef unsigned short u16;
typedef unsigned long long u64;
#define LAS __attribute__((address_space(3)))

constexpr int L = 2048, D = 640, NM = 128, F = 1728;

#define ST_ARG
#define STAMPC(i)
#define ST_PASS
#define STAMP_DECL
#define STAMP_END()
#define STAMP(i)

#define BAR() asm volatile("s_waitcnt lgkmcnt(0)\n\ts_barrier" ::: "memory")
#define BAR_NOWAIT() asm volatile("s_barrier" ::: "memory")
#define DRAIN() asm volatile("s_waitcnt vmcnt(0)" ::: "memory")
#define SB() __builtin_amdgcn_sched_barrier(0)

__device__ __forceinline__ float bf2f(unsigned x) { return __uint_as_float(x << 16); }
__device__ __forceinline__ f32x2 unpk(unsigned x) {
    f32x2 r; r.x = __uint_as_float(x << 16); r.y = __uint_as_float(x & 0xffff0000u); return r;
}
__device__ __forceinline__ float rbf(float x) { return (float)(__bf16)x; }
__device__ __forceinline__ unsigned f2bf(float x) { __bf16 b = (__bf16)x; return (unsigned)__builtin_bit_cast(u16, b); }
__device__ __forceinline__ unsigned pkbf(f32x2 v) {
    bf16x2 b = __builtin_convertvector(v, bf16x2); return __builtin_bit_cast(unsigned, b);
}
__device__ __forceinline__ f32x2 rbf2(f32x2 v) { return unpk(pkbf(v)); }
__device__ __forceinline__ unsigned pkh(float lo, float hi) {
    f32x2 v = {lo, hi}; f16x2 h = __builtin_convertvector(v, f16x2); return __builtin_bit_cast(unsigned, h);
}
__device__ __forceinline__ unsigned padd(unsigned a, unsigned b) {
    return __builtin_bit_cast(unsigned, __builtin_bit_cast(f16x2, a) + __builtin_bit_cast(f16x2, b));
}
__device__ __forceinline__ float dot2bf(unsigned a, unsigned b, float acc) {
    return __builtin_amdgcn_fdot2_f32_bf16(__builtin_bit_cast(bf16x2, a), __builtin_bit_cast(bf16x2, b), acc, false);
}
__device__ __forceinline__ f16x4 trread(const char* p) {
    return __builtin_bit_cast(f16x4, __builtin_amdgcn_ds_read_tr16_b64_v4i16((LAS s16x4*)p));
}
__device__ __forceinline__ f16x8 cat(f16x4 a, f16x4 b) { return __builtin_shufflevector(a, b, 0, 1, 2, 3, 4, 5, 6, 7); }

template <int CTRL, int RM = 0xf>
__device__ __forceinline__ float dppf(float v) {
    return __builtin_bit_cast(float, __builtin_amdgcn_update_dpp(0, __builtin_bit_cast(int, v), CTRL, RM, 0xf, false));
}
template <int CTRL, int RM = 0xf>
__device__ __forceinline__ unsigned dppu(unsigned v) {
    return (unsigned)__builtin_amdgcn_update_dpp(0, (int)v, CTRL, RM, 0xf, false);
}
__device__ __forceinline__ float row16_sum(float v) {
    v += dppf<0xB1>(v); v += dppf<0x4E>(v); v += dppf<0x141>(v); v += dppf<0x140>(v);
    return v;
}
__device__ __forceinline__ float wave_sum_u(float v) {
    v = row16_sum(v);
    v += dppf<0x142, 0xa>(v);
    v += dppf<0x143, 0xc>(v);
    return __builtin_bit_cast(float, __builtin_amdgcn_readlane(__builtin_bit_cast(int, v), 63));
}
__device__ __forceinline__ unsigned wave_max_u(unsigned v) {
    v = max(v, dppu<0xB1>(v)); v = max(v, dppu<0x4E>(v)); v = max(v, dppu<0x141>(v)); v = max(v, dppu<0x140>(v));
    v = max(v, dppu<0x142, 0xa>(v)); v = max(v, dppu<0x143, 0xc>(v));
    return (unsigned)__builtin_amdgcn_readlane((int)v, 63);
}
__device__ __forceinline__ f32x2 cmul(f32x2 a, f32x2 b) {
    f32x2 o;
    asm("v_pk_mul_f32 %0, %1, %2 op_sel_hi:[0,1]\n\t"
        "v_pk_fma_f32 %0, %1, %2, %0 op_sel:[1,1,0] op_sel_hi:[1,0,1] neg_lo:[0,1,0]"
        : "=&v"(o) : "v"(a), "v"(b));
    return o;
}
__device__ __forceinline__ float exp_acc(float x) {
    const float l2e = 1.44269504088896341f, l2e_lo = 1.92596299e-8f;
    const float t = x * l2e;
    float e = fmaf(x, l2e, -t);
    e = fmaf(x, l2e_lo, e);
    const float r = __builtin_amdgcn_exp2f(t);
    return fmaf(r * e, 0.693147180559945f, r);
}
__device__ __forceinline__ float sigmoid_f(float x) {
    const float e = __builtin_amdgcn_exp2f(fminf(-x * 1.44269504088896341f, 126.f));
    return __builtin_amdgcn_rcpf(1.f + e);
}
__device__ __forceinline__ f32x2 cexp_p(float zl, float wh, float wl, float p) {
    const float mag = __builtin_amdgcn_exp2f(zl * p);
    float t = __builtin_amdgcn_fractf(wh * p);
    t = fmaf(wl, p, t);
    f32x2 o = {__builtin_amdgcn_cosf(t), __builtin_amdgcn_sinf(t)};
    return o * mag;
}
__device__ __forceinline__ void ln_finish(float piv, float s1, float s2, float& mean_b, float& rstd) {
    const float dm = s1 * (1.f / D);
    const float var = fmaxf(fmaf(-dm, dm, s2 * (1.f / D)), 0.f);
    mean_b = rbf(piv + dm);
    rstd = __builtin_amdgcn_rcpf(rbf(__builtin_amdgcn_sqrtf(rbf(rbf(var) + 1e-5f))));
}
__device__ __forceinline__ void row_stats(const f32x2 (&v)[5], float& mb, float& rs) {
    const float piv = __builtin_bit_cast(float, __builtin_amdgcn_readfirstlane(__builtin_bit_cast(int, v[0].x)));
    f32x2 s1 = v[0] - piv, s2 = s1 * s1;
#pragma unroll
    for (int i = 1; i < 5; ++i) { f32x2 d = v[i] - piv; s1 += d; s2 = __builtin_elementwise_fma(d, d, s2); }
    auto sw = __builtin_amdgcn_permlane32_swap(__float_as_uint(s1.x + s1.y), __float_as_uint(s2.x + s2.y), false, false);
    float t = row16_sum(__uint_as_float(sw[0]) + __uint_as_float(sw[1]));
    t += dppf<0x142, 0xa>(t);
    const float t1 = __builtin_bit_cast(float, __builtin_amdgcn_readlane(__builtin_bit_cast(int, t), 31));
    const float t2 = __builtin_bit_cast(float, __builtin_amdgcn_readlane(__builtin_bit_cast(int, t), 63));
    ln_finish(piv, t1, t2, mb, rs);
}

constexpr unsigned WS_EPOCH = 0;
#define FSTR_ 64
constexpr unsigned FSTR = FSTR_;
constexpr unsigned WS_FLAGS = 4096;
constexpr unsigned WS_FLAGS2 = WS_FLAGS + 256 * FSTR;
constexpr unsigned WS_KEYS = WS_FLAGS2 + 256 * FSTR;
constexpr unsigned WS_REC = 131072;
constexpr unsigned WS_Y = 2u << 20;
constexpr unsigned WS_UT = 5u << 20;
constexpr unsigned WS_END = 8u << 20;
constexpr unsigned SENT = 0xffffffffu;
constexpr int SPIN_MAX = 20000;

constexpr int MSTR = 272, TABSZ = NM * MSTR;
constexpr int WSTR = 3200, WSLOT = 4 * WSTR;
constexpr int WG = 3 * WSLOT;
constexpr int KB = 3 * TABSZ, KCS = 1344, T2 = 4 * KCS, T3 = T2 + 79 * 16, KSLOT = T3 + 256;
constexpr int UB = KB + 3 * KSLOT, UOFF = 17 * 80, USLOT = 81 * 80;
constexpr int TRB = UB + 3 * USLOT, RSTR = 1344;
constexpr int MISC = TRB + 8 * RSTR;
constexpr int LDS_BYTES = MISC + 1024;
static_assert(WG + 40960 <= KB && LDS_BYTES <= 163840 && KSLOT % 16 == 0, "lds map");

typedef __amdgpu_buffer_rsrc_t rsrc_t;
__device__ __forceinline__ void st16(rsrc_t r, unsigned off, u32x4 v) { __builtin_amdgcn_raw_buffer_store_b128(v, r, off, 0, 16); }
__device__ __forceinline__ void st8(rsrc_t r, unsigned off, u32x2 v) { __builtin_amdgcn_raw_buffer_store_b64(v, r, off, 0, 16); }
__device__ __forceinline__ void st4(rsrc_t r, unsigned off, unsigned v) { __builtin_amdgcn_raw_buffer_store_b32(v, r, off, 0, 16); }
__device__ __forceinline__ u32x4 ld16(rsrc_t r, unsigned off) { return __builtin_amdgcn_raw_buffer_load_b128(r, off, 0, 16); }
__device__ __forceinline__ u32x2 ld8(rsrc_t r, unsigned off) { return __builtin_amdgcn_raw_buffer_load_b64(r, off, 0, 16); }
__device__ __forceinline__ unsigned ld4(rsrc_t r, unsigned off) { return __builtin_amdgcn_raw_buffer_load_b32(r, off, 0, 16); }
__device__ __forceinline__ bool pending(u32x4 v) { return max(max(v.x, v.y), max(v.z, v.w)) == SENT; }
__device__ __forceinline__ u32x4 poll16(rsrc_t r, unsigned off, u32x4 v) {
    for (int s = 0; s < SPIN_MAX; ++s) {
        const bool pend = pending(v);
        if (!__any(pend)) break;
        __builtin_amdgcn_s_sleep(2);
        asm volatile("" ::: "memory");
        if (pend) v = ld16(r, off);
    }
    return v;
}
template <typename BAD>
__device__ __forceinline__ void sweep4(rsrc_t r, unsigned base, int lane, unsigned (&f)[4], BAD bad) {
    const unsigned off = base + lane * FSTR;
#pragma unroll
    for (int k = 0; k < 4; ++k) f[k] = ld4(r, off + k * 64 * FSTR);
    for (int s = 0; s < SPIN_MAX; ++s) {
        if (!__any(bad(f[0]) || bad(f[1]) || bad(f[2]) || bad(f[3]))) break;
        __builtin_amdgcn_s_sleep(2);
        asm volatile("" ::: "memory");
#pragma unroll
        for (int k = 0; k < 4; ++k) if (bad(f[k])) f[k] = ld4(r, off + k * 64 * FSTR);
    }
}
__device__ __forceinline__ unsigned poll_keys(rsrc_t r, int lane) {
    unsigned f[4];
    sweep4(r, WS_KEYS, lane, f, [](unsigned v) { return v == SENT; });
    return wave_max_u(max(max(f[0], f[1]), max(f[2], f[3])));
}

#define MFMA32(a, b, c) __builtin_amdgcn_mfma_f32_32x32x16_f16(a, b, c, 0, 0, 0)

struct ConvPtr { const char *an, *bn, *a2, *b2, *a3, *b3; };
template <int TILE> struct Steps {
    static constexpr int NN = TILE ? 38 : 35, N2 = TILE ? 24 : 8, N3 = TILE ? 8 : 0, N = NN + N2 + N3;
};
template <int TILE, int S>
__device__ __forceinline__ void conv_ld(const ConvPtr& P, f16x8& A, f16x8& B) {
    using St = Steps<TILE>;
    if constexpr (S < St::NN) {
        constexpr int n = S - 1, nb = n > 33 ? n + 29 : n;
        constexpr int boff = (TILE ? 33 : 17) * 80 + ((nb & 1) ? -((nb + 1) / 2) * 80 + 32 : -(nb / 2) * 80);
        A = cat(trread(P.an + S * 32 + 8), trread(P.an + S * 32));
        B = *(const f16x8*)(P.bn + boff);
    } else if constexpr (S < St::NN + St::N2) {
        constexpr int s = S - St::NN;
        A = cat(trread(P.a2 + s * 128 + 32), trread(P.a2 + s * 128));
        B = *(const f16x8*)(P.b2 + (St::N2 - 1 - s) * 32);
    } else {
        constexpr int s = S - St::NN - St::N2;
        A = cat(trread(P.a3 + s * 256), trread(P.a3 + s * 256 + 64));
        B = cat(*(const f16x4*)(P.b3 + (7 - s) * 32 + 8), *(const f16x4*)(P.b3 + (7 - s) * 32));
    }
}
constexpr int PF = 4;
template <int TILE, int S>
__device__ __forceinline__ void conv_steps(const ConvPtr& P, f16x8 (&A)[PF], f16x8 (&B)[PF], f32x16& acc,
                                           volatile LAS unsigned* cnt, unsigned& seen ST_ARG) {
    using St = Steps<TILE>;
    if constexpr (S < St::N) {
        acc = MFMA32(A[S % PF], B[S % PF], acc);
        if constexpr (S + PF + 4 == St::NN) seen = *cnt;
        if constexpr (S + PF == St::NN) {
            SB();
            for (int s = 0; s < SPIN_MAX && __builtin_amdgcn_readfirstlane(seen) < 8u; ++s) seen = *cnt;
            asm volatile("" ::: "memory");
        }
        if constexpr (S + PF < St::N) conv_ld<TILE, S + PF>(P, A[S % PF], B[S % PF]);
        SB();
        conv_steps<TILE, S + 1>(P, A, B, acc, cnt, seen ST_PASS);
    }
}
__device__ __forceinline__ ConvPtr conv_setup(char* smem, int slot, int tile, int lane) {
    const int c = lane & 31, h = lane >> 5, q = (lane >> 2) & 3;
    const int G = ((lane >> 4) & 1) * 4 + (lane & 3);
    const int tr = 16 * (G & 1) + 4 * (G >> 1);
    const char* wsl = smem + slot * WSLOT;
    const char* ks = smem + KB + slot * KSLOT;
    ConvPtr P;
    P.an = ks + q * KCS + 2 * (tr - 8 * h + 8);
    P.bn = smem + UB + slot * USLOT + UOFF + (c - (tile ? 1 : 17)) * 80 + 16 * h;
    P.a2 = wsl + (q & 1) * WSTR + 2 * (36 + 32 * h + tr - 4 * (q + 1) - 16);
    P.b2 = ks + T2 + (1 + c - h) * 16;
    P.a3 = wsl + (2 + (q & 1)) * WSTR + 2 * (36 + 64 * h + tr - 8 * (q + 1));
    P.b3 = ks + T3 + (c - 2 * h - 29) * 8;
    return P;
}
template <int TILE>
__device__ __forceinline__ void conv_tile(char* smem, const ConvPtr& P, int slot, unsigned yo, rsrc_t wsr ST_ARG) {
    f16x8 A[PF], B[PF];
    conv_ld<TILE, 0>(P, A[0], B[0]); conv_ld<TILE, 1>(P, A[1], B[1]);
    conv_ld<TILE, 2>(P, A[2], B[2]); conv_ld<TILE, 3>(P, A[3], B[3]);
    const float sc = *(const float*)(smem + MISC + 96 + slot * 4);
    f32x16 acc;
#pragma unroll
    for (int i = 0; i < 16; ++i) acc[i] = 0.f;
    SB();
    unsigned seen = 0;
    conv_steps<TILE, 0>(P, A, B, acc, (volatile LAS unsigned*)(smem + MISC + 48), seen ST_PASS);
#pragma unroll
    for (int g = 0; g < 2; ++g) {
        u32x4 o;
#pragma unroll
        for (int e = 0; e < 4; ++e) o[e] = pkbf((f32x2){acc[8 * g + 2 * e], acc[8 * g + 2 * e + 1]} * sc);
        st16(wsr, yo + g * 1024, o);
    }
}

template <bool FULL>
__device__ __forceinline__ void conv_windows(char* smem, int slot, int i, int lane) {
    const char* op = smem + UB + slot * USLOT + UOFF + (lane >> 2) * 80 + (lane & 3) * 16 + i * 1280;
    const u32x4 own = *(const u32x4*)op;
    const u32x4 pv = *(const u32x4*)(op - ((lane & 3) ? 16 : 32));
    unsigned w[8] = {pv.x, pv.y, pv.z, pv.w, own.x, own.y, own.z, own.w};
    unsigned a[7];
#pragma unroll
    for (int k = 0; k < 7; ++k) a[k] = __builtin_amdgcn_alignbit(w[k + 1], w[k], 16);
    u32x4 w3, w5, w7, w9;
#pragma unroll
    for (int k = 0; k < 4; ++k) {
        w3[k] = padd(w[2 + k], padd(a[1 + k], a[2 + k]));
        w5[k] = padd(w3[k], padd(w[1 + k], w[3 + k]));
        if (FULL) {
            w7[k] = padd(w5[k], padd(a[k], a[3 + k]));
            w9[k] = padd(w7[k], padd(w[k], w[4 + k]));
        }
    }
    char* wp = smem + slot * WSLOT + 64 + lane * 16 + i * 1024;
    *(u32x4*)(wp) = w3;
    *(u32x4*)(wp + WSTR) = w5;
    if (FULL) {
        *(u32x4*)(wp + 2 * WSTR) = w7;
        *(u32x4*)(wp + 3 * WSTR) = w9;
    }
}

#define SLP_FLAG 6
#define SLP_F2 16
#define SLP_U 12
#define SLP_U2 2
#define FIN_DLY 125
#define SLP_Y -1
#define SLP_FIN 28
#define SLP_FK 24
#define SLP_CLR 12
#define SLP_KEYS 16
#define SLP_W 12
__device__ __forceinline__ void lds_add1(char* q) {
    const unsigned a = (unsigned)(size_t)(LAS char*)q, one = 1u;
    asm volatile("ds_add_u32 %0, %1" :: "v"(a), "v"(one) : "memory");
}

__device__ __forceinline__ void lds_wait(char* q, unsigned n) {
    for (int s = 0; s < SPIN_MAX; ++s) {
        if (__builtin_amdgcn_readfirstlane(*(volatile LAS unsigned*)q) >= n) break;
        __builtin_amdgcn_s_sleep(1);
    }
    asm volatile("" ::: "memory");
}
#define LGKM0() asm volatile("s_waitcnt lgkmcnt(0)" ::: "memory")
#define RB2(v) rbf2(v)

extern "C" __global__ void __launch_bounds__(1024, 4)
fused_kernel(const u16* __restrict__ x, const u16* __restrict__ logA, const u16* __restrict__ Aim,
             const u16* __restrict__ Cre, const u16* __restrict__ Cim, const u16* __restrict__ logdt,
             const u16* __restrict__ dskip, const u16* __restrict__ wr, const u16* __restrict__ wg,
             const u16* __restrict__ wu, const u16* __restrict__ wd, u16* out, char* ws) {
    extern __shared__ __attribute__((aligned(16))) char smem[];
    asm volatile("" :: "s"(x), "s"(logA), "s"(Aim), "s"(Cre), "s"(Cim), "s"(logdt), "s"(dskip), "s"(wr), "s"(wg), "s"(wu),
                 "s"(wd), "s"(out), "s"(ws));
    const int tid = threadIdx.x, lane = tid & 63;
    const int wave = __builtin_amdgcn_readfirstlane(tid >> 6);
    const int p = (blockIdx.x & 7) * 32 + (blockIdx.x >> 3);
    const int isB = p & 1;
    const int d0 = 5 * (p >> 1) + 2 * isB;
    const int fs = (p >> 5) * 14 + (p & 31);
    const bool ffn = (p & 31) < 14 && fs < 108;
    const rsrc_t wsr = __builtin_amdgcn_make_buffer_rsrc(ws, 0, WS_END, 0x00020000);
    const u32x4 s4 = {SENT, SENT, SENT, SENT};
    STAMP_DECL; STAMP(0);

    auto ld5 = [&](const u16* q_, unsigned (&o)[5]) {
        u32x2 a = *(const u32x2*)(q_ + lane * 4), b = *(const u32x2*)(q_ + 256 + lane * 4);
        o[0] = a.x; o[1] = a.y; o[2] = b.x; o[3] = b.y; o[4] = *(const unsigned*)(q_ + 512 + lane * 2);
    };
    auto chof = [&](int s) { return isB ? d0 + (s == 2 ? 0 : s + 1) : d0 + s; };
    const int row = p * 8 + (wave & 7);
    const int sub = wave & 3;
    unsigned xr[5];
    unsigned tag = 0, cpf = 0;
    u32x4 tg = {0u, 0u, 0u, 0u};
    float mb1 = 0.f, rs1 = 0.f;
    if (wave >= 8) {
        const int slot = wave >= 12 ? 1 + (sub >> 1) : 0;
        const bool doA = wave >= 10, doB = wave >= 12 || wave < 10;
        const int dch = chof(slot);
        const unsigned ldt_ = ((const unsigned*)logdt)[dch >> 1];
        const unsigned la_ = ((const unsigned*)(logA + dch * NM))[lane];
        const unsigned ai_ = ((const unsigned*)(Aim + dch * NM))[lane];
        const unsigned c0 = ((const unsigned*)(Cre + dch * NM))[lane];
        const unsigned c1 = ((const unsigned*)(Cim + dch * NM))[lane];
        BAR_NOWAIT();
        if (wave >= 12) __builtin_amdgcn_s_setprio(2);
        const unsigned sh = (wave & 1) ? 0u : 16u;
        auto pick = [&](unsigned w) { return __uint_as_float((w << sh) & 0xffff0000u); };
        const float dt = rbf(exp_acc(__uint_as_float((dch & 1) ? ldt_ & 0xffff0000u : ldt_ << 16)));
        const float zr = -rbf(exp_acc(pick(la_))) * dt;
        const float zi = pick(ai_) * dt;
        const float zl = zr * 1.44269504088896341f;
        const float wh = zi * 0.15915494309189535f;
        const float wl = fmaf(zi, 6.4206383e-09f, fmaf(zi, 0.15915494309189535f, -wh));
        STAMP(1);
        const f32x2 e8 = cexp_p(zl, wh, wl, 8.f);
        const f32x2 e128 = cexp_p(zl, wh, wl, 128.f);
        const f32x2 e256 = cmul(e128, e128), e384 = cmul(e128, e256), e512 = cmul(e256, e256);
        char* tb = smem + slot * TABSZ + ((wave & 1) * 64 + lane) * MSTR;
        auto put8 = [&](int grp, const f32x2 (&e)[8]) {
            u32x4 re = {pkh(e[0].x, e[1].x), pkh(e[2].x, e[3].x), pkh(e[4].x, e[5].x), pkh(e[6].x, e[7].x)};
            u32x4 im = {pkh(e[0].y, e[1].y), pkh(e[2].y, e[3].y), pkh(e[4].y, e[5].y), pkh(e[6].y, e[7].y)};
            *(u32x4*)(tb + grp * 16) = re;
            *(u32x4*)(tb + 128 + grp * 16) = im;
        };
        if (doA) {
            f32x2 e[8], g[8];
            e[0] = (f32x2){1.f, 0.f};
            e[1] = cexp_p(zl, wh, wl, 1.f);
            e[2] = cmul(e[1], e[1]); e[4] = cmul(e[2], e[2]);
            e[3] = cmul(e[1], e[2]); e[5] = cmul(e[1], e[4]); e[6] = cmul(e[2], e[4]); e[7] = cmul(e[3], e[4]);
            put8(0, e);
            g[0] = e128;
#pragma unroll
            for (int r = 1; r < 8; ++r) g[r] = cmul(e[r], e128);
            put8(1, g);
            g[0] = cmul(e[2], e256); g[1] = cmul(e[4], e256); g[2] = cmul(e[6], e256); g[3] = cmul(e8, e256);
            g[4] = cmul(e[2], e384); g[5] = cmul(e[4], e384); g[6] = cmul(e[6], e384); g[7] = cmul(e8, e384);
            put8(2, g);
            g[0] = cmul(e512, e[4]); g[1] = cmul(e512, e8);
            g[2] = cmul(g[0], e128); g[3] = cmul(g[1], e128);
            g[4] = cmul(g[0], e256); g[5] = cmul(g[1], e256);
            g[6] = cmul(g[0], e384); g[7] = cmul(g[1], e384);
            put8(3, g);
        }
        if (doB) {
            const unsigned m0 = c0 & 0x7fff7fffu, m1 = c1 & 0x7fff7fffu;
            const unsigned mx = wave_max_u(max(max(m0 >> 16, m0 & 0xffffu), max(m1 >> 16, m1 & 0xffffu)));
            const int ex = min(max((int)(mx >> 7), 8), 246);
            const float up = __uint_as_float((unsigned)(260 - ex) << 23);
            if (lane == 0) *(float*)(smem + MISC + 96 + slot * 4) = 2.f * dt * __uint_as_float((unsigned)(ex - 6) << 23);
            const f32x2 c8 = {e8.x, -e8.y};
            const f32x2 c16 = cmul(c8, c8), c32 = cmul(c16, c16), c64 = cmul(c32, c32);
            const f32x2 e1024 = cmul(e512, e512);
            f32x2 b[8], g[8];
            g[0] = cmul(e1024, e8); g[1] = cmul(g[0], e128); g[2] = cmul(g[0], e256); g[3] = cmul(g[0], e384);
#pragma unroll
            for (int s = 0; s < 4; ++s) g[4 + s] = cmul(g[s], e512);
            put8(4, g);
            *(u32x2*)(tb + 80) = (u32x2){pkh(e256.x, e512.x), pkh(e1024.x, 0.f)};
            *(u32x2*)(tb + 128 + 80) = (u32x2){pkh(e256.y, e512.y), pkh(e1024.y, 0.f)};
            b[0] = (f32x2){pick(c0) * up, -pick(c1) * up};
            b[1] = cmul(b[0], c8); b[2] = cmul(b[0], c16); b[3] = cmul(b[1], c16);
#pragma unroll
            for (int j = 0; j < 4; ++j) b[4 + j] = cmul(b[j], c32);
            put8(6, b);
#pragma unroll
            for (int j = 0; j < 8; ++j) g[j] = cmul(b[j], c64);
            put8(7, g);
        }
        LGKM0();
        if (lane == 0) lds_add1(smem + MISC + 64 + slot * 4);
        __builtin_amdgcn_s_setprio(0);
        STAMP(4);
        } else {
        ld5(x + (size_t)row * D, xr);
        tag = ld4(wsr, WS_EPOCH);
        {
            u64 pc; unsigned off, len;
            asm volatile("s_getpc_b64 %0\n.Lfk_pc:" : "=s"(pc));
            asm volatile("s_mov_b32 %0, .Lfk_pc-fused_kernel\n\ts_mov_b32 %1, .Lfk_end-fused_kernel" : "=s"(off), "=s"(len));
            const rsrc_t cr = __builtin_amdgcn_make_buffer_rsrc((void*)(pc - off), 0, len, 0x00020000);
            cpf = __builtin_amdgcn_raw_buffer_load_b32(cr, tid * 128, 0, 0);
        }
        if (lane < 4) *(u32x4*)(smem + MISC + 32 + lane * 16) = (u32x4){0u, 0u, 0u, 0u};
        char* tp = smem + TRB + wave * RSTR + lane * 4;
        const char* tp2 = smem + TRB + ((lane >> 2) & 3) * RSTR + ((lane >> 4) * 16 + (lane & 3) * 4) * 2 + wave * 128;
        const unsigned uso = WS_UT + (p * D + tid) * 16;
        BAR();
        {
            const int c1_ = 512 + wave * 16 + lane;
            const unsigned yb = WS_Y + d0 * 4096;
            st16(wsr, yb + (tid + ((isB && tid >= 128) ? 128 : 0)) * 16, s4);
            st16(wsr, lane < 16 ? yb + (c1_ + 128) * 16 : WS_END, s4);
        }
        SB();
        __builtin_amdgcn_s_setprio(3);
        f32x2 v[5];
#pragma unroll
        for (int i = 0; i < 5; ++i) v[i] = unpk(xr[i]);
        STAMP(1);
        row_stats(v, mb1, rs1);
        unsigned uh[5];
#pragma unroll
        for (int i = 0; i < 5; ++i) {
            const f32x2 u = RB2(RB2(v[i] - mb1) * rs1);
            uh[i] = pkh(u.x, u.y);
        }
        *(u32x2*)(tp + lane * 4) = (u32x2){uh[0], uh[1]};
        *(u32x2*)(tp + 512 + lane * 4) = (u32x2){uh[2], uh[3]};
        *(unsigned*)(tp + 1024) = uh[4];
        tag = __builtin_amdgcn_readfirstlane(tag) + 1u;
        asm volatile("s_waitcnt vmcnt(0) lgkmcnt(0)" : "+s"(tag) :: "memory");
        if (lane == 0) lds_add1(smem + MISC + 52);
#pragma unroll
        for (int i = 0; i < 4; ++i) {
            const unsigned f6 = i ? (tag >> (6 * i)) + tag : tag;
            tg[i] = (f6 & 7u) | ((f6 & 56u) << 13);
        }
        if (wave < 4) {
            lds_wait(smem + MISC + 52, 8u);
            STAMP(2);
#pragma unroll
            for (int i = 0; i < 3; ++i) {
                if (i < 2 || wave < 2) {
                    u32x4 o = __builtin_bit_cast(u32x4, cat(trread(tp2 + i * 512), trread(tp2 + i * 512 + 4 * RSTR)));
                    o = (o & 0xfff8fff8u) | tg;
                    st16(wsr, uso + i * 4096, o);
                }
            }
        }
        __builtin_amdgcn_s_setprio(0);
        STAMP(3);
    }
    if (wave >= 8 || wave == 3) {
        const int ts = wave == 3 ? 2 : (wave == 11 ? 1 : (wave >= 12 ? 1 + (sub >> 1) : 0));
        const int tt = wave >= 12 ? (sub & 1) : (sub == 3 ? 2 : sub);
        char* ks = smem + KB + ts * KSLOT;
        const int qq = lane >> 4, q = (lane >> 2) & 3, pp = lane & 3, j = lane & 15;
        const char* ap = smem + ts * TABSZ + (4 * qq + 2 * (q >> 1)) * MSTR + (q & 1) * 128 + pp * 8;
        const char* bp = ap + (tt == 2 ? 64 : 96);
        ap += tt == 2 ? 96 : tt * 32;
        lds_wait(smem + MISC + 64 + ts * 4, ts ? 6u : 8u);
        STAMP(5);
        f16x8 av[8], bv[8];
#pragma unroll
        for (int k = 0; k < 8; ++k) {
            av[k] = cat(trread(ap + k * 16 * MSTR), trread(ap + k * 16 * MSTR + MSTR));
            bv[k] = cat(trread(bp + k * 16 * MSTR), trread(bp + k * 16 * MSTR + MSTR));
        }
        SB();
        f32x4 acc = {0.f, 0.f, 0.f, 0.f}, acc1 = {0.f, 0.f, 0.f, 0.f};
#pragma unroll
        for (int k = 0; k < 4; ++k) {
            acc = __builtin_amdgcn_mfma_f32_16x16x32_f16(av[2 * k], bv[2 * k], acc, 0, 0, 0);
            acc1 = __builtin_amdgcn_mfma_f32_16x16x32_f16(av[2 * k + 1], bv[2 * k + 1], acc1, 0, 0, 0);
        }
        acc += acc1;
        const unsigned p01 = pkh(acc[0], acc[1]), p23 = pkh(acc[2], acc[3]), p12 = pkh(acc[1], acc[2]);
        if (tt == 0) {
            char* o = ks + 2 * (28 + 4 * (qq & 1) + 128 * (qq >> 1) + 8 * j);
            *(u32x2*)o = (u32x2){p01, p23};
            *(unsigned*)(o + 2 * KCS + 4) = p01; *(unsigned*)(o + 2 * KCS + 8) = p23;
            *(u16*)(o + KCS + 2) = (u16)p01; *(unsigned*)(o + KCS + 4) = p12; *(u16*)(o + KCS + 8) = (u16)(p23 >> 16);
            *(u16*)(o + 3 * KCS + 6) = (u16)p01; *(unsigned*)(o + 3 * KCS + 8) = p12; *(u16*)(o + 3 * KCS + 12) = (u16)(p23 >> 16);
        } else if (tt == 1) {
            if (qq < 2) {
                const unsigned p11 = pkh(acc[1], acc[1]), p33 = pkh(acc[3], acc[3]);
                char* o = ks + 2 * (28 + 258 + 128 * qq + 8 * j);
                unsigned* o0 = (unsigned*)o;
                o0[0] = p01; o0[1] = p11; o0[2] = p23; o0[3] = p33;
                *(u32x4*)(o + 2 * KCS + 4) = (u32x4){p01, p11, p23, p33};
                *(u16*)(o + KCS + 2) = (u16)p01; *(u32x2*)(o + KCS + 4) = (u32x2){p11, p12};
                *(unsigned*)(o + KCS + 12) = p33; *(u16*)(o + KCS + 16) = (u16)p33;
                *(u16*)(o + 3 * KCS + 6) = (u16)p01; *(unsigned*)(o + 3 * KCS + 8) = p11;
                *(u32x2*)(o + 3 * KCS + 12) = (u32x2){p12, p33}; *(u16*)(o + 3 * KCS + 20) = (u16)p33;
            } else {
                unsigned* t2 = (unsigned*)(ks + T2 + 31 * 16) + 32 * (qq - 2) + j;
                t2[0] = p01; t2[16] = p23;
            }
        } else {
            if (j < 8) *(u32x2*)(ks + T3 + (16 * j + 4 * qq) * 2) = (u32x2){p01, p23};
            const unsigned s256 = __builtin_amdgcn_readlane((int)p01, 8), s512 = __builtin_amdgcn_readlane((int)p01, 9),
                           s1024 = __builtin_amdgcn_readlane((int)p01, 10);
            const int w = lane >> 2, cq = lane & 3;
            const int lg = w < 2 ? 256 + w : (w == 2 ? 514 : 560 + w);
            const unsigned sv = w < 2 ? s256 : (w == 2 ? s512 : s1024);
            if (w < 5) *(u16*)(ks + cq * KCS + 2 * (lg + cq + 28)) = (u16)sv;
        }
        if (wave >= 8 && wave < 12) {
            const int xi = tid - 512;
            const int xg = p >> 5, li = (p & 31) * 160 + xi;
            const u16* src;
            if (xi < 160) src = ((li & 4) ? wu : wg) + (size_t)(li >> 3) * F + min(((7 * xg) >> 1) + (li & 3), 26) * 64;
            else src = wd + (size_t)min(xg * 2240 + (p & 31) * 70 + min(xi - 160, 69), F * 10 - 1) * 64;
            cpf = *(const unsigned*)src;
        }
        STAMP(6);
    } else if (wave < 3) {
        if (wave == 2) {
            __builtin_amdgcn_s_sleep(SLP_CLR);
#pragma unroll
            for (int i = 0; i < 5; ++i) if (i < 3 || ffn) st16(wsr, WS_REC + p * 5120 + (i * 64 + lane) * 16, s4);
            if (lane == 0) st4(wsr, WS_KEYS + p * FSTR, SENT);
            DRAIN();
        }
        STAMP(6);
    } else {
        {
            const u32x4 z = {0u, 0u, 0u, 0u};
            const int zi = tid - 256;
#pragma unroll
            for (int i = 0; i < 6; ++i) if (i < 5 || zi < 3 * KSLOT / 16 - 1280) *(u32x4*)(smem + KB + (i * 256 + zi) * 16) = z;
            if (zi < 255) { const int s_ = zi / 85; *(u32x4*)(smem + UB + s_ * USLOT + (zi - 85 * s_) * 16) = z; }
            LGKM0();
            if (lane < 3) lds_add1(smem + MISC + 64 + lane * 4);
        }
        unsigned uo[3], lo[3];
#pragma unroll
        for (int i = 0; i < 3; ++i) {
            const int idx = i * 64 + lane, g0 = idx / 3, uc = idx - 3 * g0, ug = (wave - 4) * 64 + g0;
            const int us = isB ? (uc == 0 ? 2 : uc - 1) : uc;
            uo[i] = WS_UT + (ug * D + d0 + uc) * 16;
            lo[i] = UB + us * USLOT + UOFF + (ug >> 2) * 80 + (ug & 3) * 16;
        }
        __builtin_amdgcn_s_sleep(SLP_U);
        STAMP(4);
        u32x4 uv[3];
        for (int s = 0; s < SPIN_MAX; ++s) {
            asm volatile("" ::: "memory");
#pragma unroll
            for (int i = 0; i < 3; ++i) uv[i] = ld16(wsr, uo[i]);
            unsigned bad = 0;
#pragma unroll
            for (int i = 0; i < 3; ++i) {
                const u32x4 t = uv[i] ^ tg;
                bad |= t.x | t.y | t.z | t.w;
            }
            if (!__any((bad & 0x00070007u) != 0u)) break;
            __builtin_amdgcn_s_sleep(SLP_U2);
        }
#pragma unroll
        for (int i = 0; i < 3; ++i) *(u32x4*)(smem + lo[i]) = uv[i] & 0xfff8fff8u;
        STAMP(6);
    }
    const int cslot = wave == 11 ? 0 : (wave == 15 ? 1 : wave - 8);
    const int ctile = (sub == 3 || (wave == 10 && isB)) ? 0 : 1;
    const ConvPtr CP = conv_setup(smem, cslot, ctile, lane);
    const unsigned cyo = WS_Y + (chof(cslot) * 256 + ctile * 128 + lane) * 16;
    STAMP(7);
    BAR();
    STAMP(8);
    unsigned wdp[16], er[5], wv[5];
    if (wave < 8) {
        ld5(dskip, er);
        ld5(wr, wv);
        if (tid == 448 && p == 0) st4(wsr, WS_EPOCH, tag);
        if (wave < 6) conv_windows<true>(smem, wave >> 1, wave & 1, lane);
        else {
            conv_windows<false>(smem, wave - 6, 2, lane);
            if (wave == 6) conv_windows<false>(smem, 2, 2, lane);
            else if (lane < 48) *(u32x4*)(smem + (lane >> 2) * WSTR + (lane & 3) * 16) = (u32x4){0u, 0u, 0u, 0u};
        }
        LGKM0();
        if (lane == 0) lds_add1(smem + MISC + 48);
        STAMP(9);
#pragma unroll
        for (int i = 0; i < 5; ++i) er[i] = pkbf(RB2(RB2(unpk(xr[i]) - mb1) * rs1) * unpk(er[i]));
        if (ffn && wave < 5) {
            const unsigned* wp = (const unsigned*)(wd + (size_t)fs * 16 * D) + tid;
#pragma unroll
            for (int i = 0; i < 16; ++i) wdp[i] = wp[i * (D / 2)];
        }
        STAMP(5);
    } else if (wave < 12 || wave == 15) {
        if (wave < 12) STAMPC(2);
        if (ctile) conv_tile<1>(smem, CP, cslot, cyo, wsr ST_PASS);
        else conv_tile<0>(smem, CP, cslot, cyo, wsr ST_PASS);
        if (wave < 12) STAMPC(3);
        STAMP(9);
        __builtin_amdgcn_s_setprio(0);
        DRAIN();
    } else if (ffn) {
        u32x4 wq[14];
        const int xi = tid - 768;
#pragma unroll
        for (int i = 0; i < 14; ++i) {
            const int ci = min(i * 192 + xi, 2559), r_ = ci >> 1;
            const u16* wsrc = r_ < D ? wg + (size_t)r_ * F : wu + (size_t)(r_ - D) * F;
            if (i < 13 || wave == 12) wq[i] = *(const u32x4*)(wsrc + fs * 16 + (ci & 1) * 8);
        }
        SB();
#pragma unroll
        for (int i = 0; i < 14; ++i) if (i < 13 || wave == 12) *(u32x4*)(smem + WG + (i * 192 + xi) * 16) = wq[i];
        STAMP(9);
    }
    asm volatile("" :: "v"(cpf));
    BAR();
    STAMP(10);
    if (wave < 10) {
        const unsigned yoff = WS_Y + (tid * 256 + (p >> 7) * 128 + (p & 1) * 64 + ((p >> 1) & 1) * 32 + ((p >> 2) & 31)) * 16;
        const u32x4 yv = poll16(wsr, yoff, ld16(wsr, yoff));
        u16* yp = (u16*)(smem + TRB) + tid;
#pragma unroll
        for (int i = 0; i < 4; ++i) {
            yp[(2 * i) * D] = (u16)yv[i];
            yp[(2 * i + 1) * D] = (u16)(yv[i] >> 16);
        }
    }
    STAMP(11);
    BAR();
    unsigned op[5];
    bool plain_out = false;
    if (wave < 8) {
        unsigned yr[5], hp[5], zp[5];
        ld5((const u16*)(smem + TRB) + wave * D, yr);
        f32x2 hv[5];
#pragma unroll
        for (int i = 0; i < 5; ++i) {
            hp[i] = pkbf(unpk(xr[i]) + RB2(unpk(yr[i]) + unpk(er[i])));
            hv[i] = unpk(hp[i]);
        }
        float mb, rs;
        row_stats(hv, mb, rs);
        float ra = 0.f;
#pragma unroll
        for (int i = 0; i < 5; ++i) {
            zp[i] = pkbf(RB2(hv[i] - mb) * rs);
            ra = dot2bf(zp[i], wv[i], ra);
        }
        const float r = wave_sum_u(ra);
        unsigned rb = f2bf(r);
        rb = (rb & 0x8000u) ? (~rb & 0xffffu) : (rb | 0x8000u);
        const unsigned key = (rb << 16) | (unsigned)(2047 - row);
        if (lane == 0) *(unsigned*)(smem + MISC + wave * 4) = key;
        BAR();
        STAMP(12);
        const u32x4 k0 = *(const u32x4*)(smem + MISC), k1 = *(const u32x4*)(smem + MISC + 16);
        const unsigned best = max(max(max(k0.x, k0.y), max(k0.z, k0.w)), max(max(k1.x, k1.y), max(k1.z, k1.w)));
        plain_out = __builtin_amdgcn_readfirstlane(best) != key;
        if (!plain_out) {
            const unsigned rec = WS_REC + p * 5120;
            if (lane == 0) st4(wsr, WS_KEYS + p * FSTR, key);
            st8(wsr, rec + lane * 8, (u32x2){zp[0], zp[1]});
            st8(wsr, rec + 512 + lane * 8, (u32x2){zp[2], zp[3]});
            st4(wsr, rec + 1024 + lane * 4, zp[4]);
        }
#pragma unroll
        for (int i = 0; i < 5; ++i) op[i] = pkbf(hv[i] + unpk(zp[i]));
        if (!plain_out) {
            const unsigned rec = WS_REC + p * 5120;
            const rsrc_t orr = __builtin_amdgcn_make_buffer_rsrc(out + (size_t)row * D, 0, D * 2, 0x00020000);
            st8(orr, lane * 8, (u32x2){op[0], op[1]});
            st8(orr, 512 + lane * 8, (u32x2){op[2], op[3]});
            st4(orr, 1024 + lane * 4, op[4]);
            DRAIN();
            st8(wsr, rec + 1280 + lane * 8, (u32x2){hp[0], hp[1]});
            st8(wsr, rec + 1280 + 512 + lane * 8, (u32x2){hp[2], hp[3]});
            st4(wsr, rec + 1280 + 1024 + lane * 4, hp[4]);
        }
    } else {
        BAR();
        STAMP(12);
    }
    if (ffn) {
        f16x8 bw[4];
        const int mat = wave >= 5, kgp = wave - 5 * mat;
        if (wave < 10) {
            const char* bp = smem + WG + mat * 20480 + (kgp * 128 + (lane >> 4) * 8 + ((lane >> 2) & 3)) * 32 + (lane & 3) * 8;
#pragma unroll
            for (int k = 0; k < 4; ++k) bw[k] = cat(trread(bp + k * 1024), trread(bp + k * 1024 + 128));
        } else if (wave == 15) {
            __builtin_amdgcn_s_sleep(SLP_KEYS);
            const unsigned bk = poll_keys(wsr, lane);
            if (lane == 0) *(unsigned*)(smem + MISC + 32) = bk;
        }
        unsigned wq[16];
        if (wave < 5) {
#pragma unroll
            for (int i = 0; i < 8; ++i) {
                wq[2 * i] = __builtin_amdgcn_perm(wdp[2 * i + 1], wdp[2 * i], 0x05040100u);
                wq[2 * i + 1] = __builtin_amdgcn_perm(wdp[2 * i + 1], wdp[2 * i], 0x07060302u);
            }
        }
        BAR();
        STAMP(13);
        if (wave < 10) {
            const unsigned bk = *(const unsigned*)(smem + MISC + 32);
            const unsigned xo = WS_REC + ((2047u - (bk & 2047u)) >> 3) * 5120 + (kgp * 128 + (lane >> 4) * 8) * 2;
            u32x4 xa[4];
#pragma unroll
            for (int k = 0; k < 4; ++k) xa[k] = ld16(wsr, xo + k * 64);
#pragma unroll
            for (int k = 0; k < 4; ++k) xa[k] = poll16(wsr, xo + k * 64, xa[k]);
            f32x4 acc = {0.f, 0.f, 0.f, 0.f};
#pragma unroll
            for (int k = 0; k < 4; ++k)
                acc = __builtin_amdgcn_mfma_f32_16x16x32_bf16(__builtin_bit_cast(bf16x8, xa[k]), __builtin_bit_cast(bf16x8, bw[k]), acc, 0, 0, 0);
            if (lane < 16) *(float*)(smem + MISC + 128 + (wave * 16 + lane) * 4) = acc[0];
        }
        BAR();
        STAMP(14);
        if (wave < 5) {
            const float* pr = (const float*)(smem + MISC + 128) + (lane & 15);
            const float g = rbf(pr[0] + pr[16] + pr[32] + pr[48] + pr[64]);
            const float u = rbf(pr[80] + pr[96] + pr[112] + pr[128] + pr[144]);
            const unsigned a = f2bf(rbf(g * sigmoid_f(g)) * u);
            const unsigned ap = a | (dppu<0xB1>(a) << 16);
            float y0 = 0.f, y1 = 0.f;
#pragma unroll
            for (int i = 0; i < 8; ++i) {
                const unsigned s = (unsigned)__builtin_amdgcn_readlane((int)ap, 2 * i);
                y0 = dot2bf(wq[2 * i], s, y0);
                y1 = dot2bf(wq[2 * i + 1], s, y1);
            }
            st8(wsr, WS_REC + p * 5120 + 2560 + tid * 8, (u32x2){__float_as_uint(y0), __float_as_uint(y1)});
            __builtin_amdgcn_s_sleep(4);
        } else {
            __builtin_amdgcn_s_sleep(14);
        }
        STAMP(15);
    } else if (wave < 8) {
        for (int i = 0; i <= wave; ++i) __builtin_amdgcn_s_sleep(6);
    } else if ((p & 31) >= 16 && (p & 31) < 26 && wave >= 14) {
        const int cq = 2 * ((p >> 5) * 10 + (p & 31) - 16) + (wave - 14);
        __builtin_amdgcn_s_sleep(SLP_KEYS);
        const unsigned bk = poll_keys(wsr, lane);
        const u64 tk = __builtin_amdgcn_s_memrealtime();
        const int wrow = 2047 - (int)(bk & 2047u);
        const unsigned zo = WS_REC + (wrow >> 3) * 5120 + cq * 8 + (lane & 1) * 1280;
        u32x2 zh = ld8(wsr, zo);
        const int f0 = min(lane, 53), f1 = f0 + 54;
        const unsigned po0 = WS_REC + ((f0 / 14) * 32 + f0 % 14) * 5120 + 2560 + cq * 16;
        const unsigned po1 = WS_REC + ((f1 / 14) * 32 + f1 % 14) * 5120 + 2560 + cq * 16;
        for (int s = 0; s < SPIN_MAX; ++s) {
            const bool pend = max(zh.x, zh.y) == SENT;
            if (!__any(pend)) break;
            __builtin_amdgcn_s_sleep(2);
            asm volatile("" ::: "memory");
            if (pend) zh = ld8(wsr, zo);
        }
        unsigned rb = bk >> 16;
        rb = (rb & 0x8000u) ? (rb & 0x7fffu) : (~rb & 0xffffu);
        const float rsel = rbf(sigmoid_f(bf2f(rb)));
        const int ce = ((lane >> 5) & 1) | ((lane >> 3) & 2);
        const unsigned zq = __builtin_amdgcn_readlane((int)zh.x, 0), zq1 = __builtin_amdgcn_readlane((int)zh.y, 0);
        const unsigned hq = __builtin_amdgcn_readlane((int)zh.x, 1), hq1 = __builtin_amdgcn_readlane((int)zh.y, 1);
        const unsigned zs = (ce & 2) ? zq1 : zq, hs = (ce & 2) ? hq1 : hq;
        const float zf = __uint_as_float((ce & 1) ? zs & 0xffff0000u : zs << 16), hf = __uint_as_float((ce & 1) ? hs & 0xffff0000u : hs << 16);
        u16* op_ = out + (size_t)wrow * D + cq * 4 + ce;
        for (int s = 0; s < 200 && __builtin_amdgcn_s_memrealtime() - tk < FIN_DLY; ++s) __builtin_amdgcn_s_sleep(1);
        STAMP(13);
        u32x4 v0 = ld16(wsr, po0), v1 = ld16(wsr, po1);
        v0 = poll16(wsr, po0, v0);
        v1 = poll16(wsr, po1, v1);
        STAMP(14);
        f32x4 sv = __builtin_bit_cast(f32x4, v0) + __builtin_bit_cast(f32x4, v1);
        if (lane >= 54) sv = (f32x4){0.f, 0.f, 0.f, 0.f};
        auto s01 = __builtin_amdgcn_permlane32_swap(__float_as_uint(sv[0]), __float_as_uint(sv[1]), false, false);
        auto s23 = __builtin_amdgcn_permlane32_swap(__float_as_uint(sv[2]), __float_as_uint(sv[3]), false, false);
        const float a01 = __uint_as_float(s01[0]) + __uint_as_float(s01[1]), a23 = __uint_as_float(s23[0]) + __uint_as_float(s23[1]);
        auto sq = __builtin_amdgcn_permlane16_swap(__float_as_uint(a01), __float_as_uint(a23), false, false);
        const float tot = row16_sum(__uint_as_float(sq[0]) + __uint_as_float(sq[1]));
        const float yv = rbf(rbf(tot) * rsel);
        if ((lane & 15) == 0) *op_ = (u16)f2bf(hf + rbf(zf + yv));
        STAMP(11);
    }
    if (plain_out) {
        u16* q_ = out + (size_t)row * D;
        *(u32x2*)(q_ + lane * 4) = (u32x2){op[0], op[1]};
        *(u32x2*)(q_ + 256 + lane * 4) = (u32x2){op[2], op[3]};
        *(unsigned*)(q_ + 512 + lane * 2) = op[4];
    }
    STAMP_END();
    asm volatile(".Lfk_end:");
}

extern "C" void kernel_launch(void* const* d_in, const int* in_sizes, int n_in, void* d_out, int out_size,
                              void* d_ws, size_t ws_size, hipStream_t stream) {
    (void)hipFuncSetAttribute((const void*)fused_kernel, hipFuncAttributeMaxDynamicSharedMemorySize, LDS_BYTES);
    fused_kernel<<<256, 1024, LDS_BYTES, stream>>>(
        (const u16*)d_in[0], (const u16*)d_in[1], (const u16*)d_in[2], (const u16*)d_in[3], (const u16*)d_in[4],
        (const u16*)d_in[5], (const u16*)d_in[6], (const u16*)d_in[8], (const u16*)d_in[9], (const u16*)d_in[10],
        (const u16*)d_in[11], (u16*)d_out, (char*)d_ws);
}
```

```cpp
#include <hip/hip_runtime.h>
#include <hip/hip_bf16.h>
#include <stdint.h>

typedef _Float16 f16;
typedef f16 f16x2 __attribute__((ext_vector_type(2)));
typedef f16 f16x4 __attribute__((ext_vector_type(4)));
typedef f16 f16x8 __attribute__((ext_vector_type(8)));
typedef short s16x4 __attribute__((ext_vector_type(4)));
typedef __bf16 bf16x2 __attribute__((ext_vector_type(2)));
typedef __bf16 bf16x8 __attribute__((ext_vector_type(8)));
typedef float f32x2 __attribute__((ext_vector_type(2)));
typedef float f32x4 __attribute__((ext_vector_type(4)));
typedef float f32x16 __attribute__((ext_vector_type(16)));
typedef unsigned u32x2 __attribute__((ext_vector_type(2)));
typedef unsigned u32x4 __attribute__((ext_vector_type(4)));
typedef unsigned short u16;
typedef unsigned long long u64;
#define LAS __attribute__((address_space(3)))

constexpr int L = 2048, D = 640, NM = 128, F = 1728;

#define ST_ARG
#define STAMPC(i)
#define ST_PASS
#define STAMP_DECL
#define STAMP_END()
#define STAMP(i)

#define BAR() asm volatile("s_waitcnt lgkmcnt(0)\n\ts_barrier" ::: "memory")
#define BAR_NOWAIT() asm volatile("s_barrier" ::: "memory")
#define DRAIN() asm volatile("s_waitcnt vmcnt(0)" ::: "memory")
#define SB() __builtin_amdgcn_sched_barrier(0)

__device__ __forceinline__ float bf2f(unsigned x) { return __uint_as_float(x << 16); }
__device__ __forceinline__ f32x2 unpk(unsigned x) {
    f32x2 r; r.x = __uint_as_float(x << 16); r.y = __uint_as_float(x & 0xffff0000u); return r;
}
__device__ __forceinline__ float rbf(float x) { return (float)(__bf16)x; }
__device__ __forceinline__ unsigned f2bf(float x) { __bf16 b = (__bf16)x; return (unsigned)__builtin_bit_cast(u16, b); }
__device__ __forceinline__ unsigned pkbf(f32x2 v) {
    bf16x2 b = __builtin_convertvector(v, bf16x2); return __builtin_bit_cast(unsigned, b);
}
__device__ __forceinline__ f32x2 rbf2(f32x2 v) { return unpk(pkbf(v)); }
__device__ __forceinline__ unsigned pkh(float lo, float hi) {
    f32x2 v = {lo, hi}; f16x2 h = __builtin_convertvector(v, f16x2); return __builtin_bit_cast(unsigned, h);
}
__device__ __forceinline__ unsigned padd(unsigned a, unsigned b) {
    return __builtin_bit_cast(unsigned, __builtin_bit_cast(f16x2, a) + __builtin_bit_cast(f16x2, b));
}
__device__ __forceinline__ float dot2bf(unsigned a, unsigned b, float acc) {
    return __builtin_amdgcn_fdot2_f32_bf16(__builtin_bit_cast(bf16x2, a), __builtin_bit_cast(bf16x2, b), acc, false);
}
__device__ __forceinline__ f16x4 trread(const char* p) {
    return __builtin_bit_cast(f16x4, __builtin_amdgcn_ds_read_tr16_b64_v4i16((LAS s16x4*)p));
}
__device__ __forceinline__ f16x8 cat(f16x4 a, f16x4 b) { return __builtin_shufflevector(a, b, 0, 1, 2, 3, 4, 5, 6, 7); }

template <int CTRL, int RM = 0xf>
__device__ __forceinline__ float dppf(float v) {
    return __builtin_bit_cast(float, __builtin_amdgcn_update_dpp(0, __builtin_bit_cast(int, v), CTRL, RM, 0xf, false));
}
template <int CTRL, int RM = 0xf>
__device__ __forceinline__ unsigned dppu(unsigned v) {
    return (unsigned)__builtin_amdgcn_update_dpp(0, (int)v, CTRL, RM, 0xf, false);
}
__device__ __forceinline__ float row16_sum(float v) {
    v += dppf<0xB1>(v); v += dppf<0x4E>(v); v += dppf<0x141>(v); v += dppf<0x140>(v);
    return v;
}
__device__ __forceinline__ float wave_sum_u(float v) {
    v = row16_sum(v);
    v += dppf<0x142, 0xa>(v);
    v += dppf<0x143, 0xc>(v);
    return __builtin_bit_cast(float, __builtin_amdgcn_readlane(__builtin_bit_cast(int, v), 63));
}
__device__ __forceinline__ unsigned wave_max_u(unsigned v) {
    v = max(v, dppu<0xB1>(v)); v = max(v, dppu<0x4E>(v)); v = max(v, dppu<0x141>(v)); v = max(v, dppu<0x140>(v));
    v = max(v, dppu<0x142, 0xa>(v)); v = max(v, dppu<0x143, 0xc>(v));
    return (unsigned)__builtin_amdgcn_readlane((int)v, 63);
}
__device__ __forceinline__ f32x2 cmul(f32x2 a, f32x2 b) {
    f32x2 o;
    asm("v_pk_mul_f32 %0, %1, %2 op_sel_hi:[0,1]\n\t"
        "v_pk_fma_f32 %0, %1, %2, %0 op_sel:[1,1,0] op_sel_hi:[1,0,1] neg_lo:[0,1,0]"
        : "=&v"(o) : "v"(a), "v"(b));
    return o;
}
__device__ __forceinline__ float exp_acc(float x) {
    const float l2e = 1.44269504088896341f, l2e_lo = 1.92596299e-8f;
    const float t = x * l2e;
    float e = fmaf(x, l2e, -t);
    e = fmaf(x, l2e_lo, e);
    const float r = __builtin_amdgcn_exp2f(t);
    return fmaf(r * e, 0.693147180559945f, r);
}
__device__ __forceinline__ float sigmoid_f(float x) {
    const float e = __builtin_amdgcn_exp2f(fminf(-x * 1.44269504088896341f, 126.f));
    return __builtin_amdgcn_rcpf(1.f + e);
}
__device__ __forceinline__ f32x2 cexp_p(float zl, float wh, float wl, float p) {
    const float mag = __builtin_amdgcn_exp2f(zl * p);
    float t = __builtin_amdgcn_fractf(wh * p);
    t = fmaf(wl, p, t);
    f32x2 o = {__builtin_amdgcn_cosf(t), __builtin_amdgcn_sinf(t)};
    return o * mag;
}
__device__ __forceinline__ void ln_finish(float piv, float s1, float s2, float& mean_b, float& rstd) {
    const float dm = s1 * (1.f / D);
    const float var = fmaxf(fmaf(-dm, dm, s2 * (1.f / D)), 0.f);
    mean_b = rbf(piv + dm);
    rstd = __builtin_amdgcn_rcpf(rbf(__builtin_amdgcn_sqrtf(rbf(rbf(var) + 1e-5f))));
}
__device__ __forceinline__ void row_stats(const f32x2 (&v)[5], float& mb, float& rs) {
    const float piv = __builtin_bit_cast(float, __builtin_amdgcn_readfirstlane(__builtin_bit_cast(int, v[0].x)));
    f32x2 s1 = v[0] - piv, s2 = s1 * s1;
#pragma unroll
    for (int i = 1; i < 5; ++i) { f32x2 d = v[i] - piv; s1 += d; s2 = __builtin_elementwise_fma(d, d, s2); }
    auto sw = __builtin_amdgcn_permlane32_swap(__float_as_uint(s1.x + s1.y), __float_as_uint(s2.x + s2.y), false, false);
    float t = row16_sum(__uint_as_float(sw[0]) + __uint_as_float(sw[1]));
    t += dppf<0x142, 0xa>(t);
    const float t1 = __builtin_bit_cast(float, __builtin_amdgcn_readlane(__builtin_bit_cast(int, t), 31));
    const float t2 = __builtin_bit_cast(float, __builtin_amdgcn_readlane(__builtin_bit_cast(int, t), 63));
    ln_finish(piv, t1, t2, mb, rs);
}

constexpr unsigned WS_EPOCH = 0;
#define FSTR_ 64
constexpr unsigned FSTR = FSTR_;
constexpr unsigned WS_FLAGS = 4096;
constexpr unsigned WS_FLAGS2 = WS_FLAGS + 256 * FSTR;
constexpr unsigned WS_KEYS = WS_FLAGS2 + 256 * FSTR;
constexpr unsigned WS_REC = 131072;
constexpr unsigned WS_Y = 2u << 20;
constexpr unsigned WS_UT = 5u << 20;
constexpr unsigned WS_END = 8u << 20;
constexpr unsigned SENT = 0xffffffffu;
constexpr int SPIN_MAX = 20000;

constexpr int MSTR = 272, TABSZ = NM * MSTR;
constexpr int WSTR = 3200, WSLOT = 4 * WSTR;
constexpr int WG = 3 * WSLOT;
constexpr int KB = 3 * TABSZ, KCS = 1344, T2 = 4 * KCS, T3 = T2 + 79 * 16, KSLOT = T3 + 256;
constexpr int UB = KB + 3 * KSLOT, UOFF = 17 * 80, USLOT = 81 * 80;
constexpr int TRB = UB + 3 * USLOT, RSTR = 1344;
constexpr int MISC = TRB + 8 * RSTR;
constexpr int LDS_BYTES = MISC + 1024;
static_assert(WG + 40960 <= KB && LDS_BYTES <= 163840 && KSLOT % 16 == 0, "lds map");

typedef __amdgpu_buffer_rsrc_t rsrc_t;
__device__ __forceinline__ void st16(rsrc_t r, unsigned off, u32x4 v) { __builtin_amdgcn_raw_buffer_store_b128(v, r, off, 0, 16); }
__device__ __forceinline__ void st8(rsrc_t r, unsigned off, u32x2 v) { __builtin_amdgcn_raw_buffer_store_b64(v, r, off, 0, 16); }
__device__ __forceinline__ void st4(rsrc_t r, unsigned off, unsigned v) { __builtin_amdgcn_raw_buffer_store_b32(v, r, off, 0, 16); }
__device__ __forceinline__ u32x4 ld16(rsrc_t r, unsigned off) { return __builtin_amdgcn_raw_buffer_load_b128(r, off, 0, 16); }
__device__ __forceinline__ u32x2 ld8(rsrc_t r, unsigned off) { return __builtin_amdgcn_raw_buffer_load_b64(r, off, 0, 16); }
__device__ __forceinline__ unsigned ld4(rsrc_t r, unsigned off) { return __builtin_amdgcn_raw_buffer_load_b32(r, off, 0, 16); }
__device__ __forceinline__ bool pending(u32x4 v) { return max(max(v.x, v.y), max(v.z, v.w)) == SENT; }
__device__ __forceinline__ u32x4 poll16(rsrc_t r, unsigned off, u32x4 v) {
    for (int s = 0; s < SPIN_MAX; ++s) {
        const bool pend = pending(v);
        if (!__any(pend)) break;
        __builtin_amdgcn_s_sleep(2);
        asm volatile("" ::: "memory");
        if (pend) v = ld16(r, off);
    }
    return v;
}
template <typename BAD>
__device__ __forceinline__ void sweep4(rsrc_t r, unsigned base, int lane, unsigned (&f)[4], BAD bad) {
    const unsigned off = base + lane * FSTR;
#pragma unroll
    for (int k = 0; k < 4; ++k) f[k] = ld4(r, off + k * 64 * FSTR);
    for (int s = 0; s < SPIN_MAX; ++s) {
        if (!__any(bad(f[0]) || bad(f[1]) || bad(f[2]) || bad(f[3]))) break;
        __builtin_amdgcn_s_sleep(2);
        asm volatile("" ::: "memory");
#pragma unroll
        for (int k = 0; k < 4; ++k) if (bad(f[k])) f[k] = ld4(r, off + k * 64 * FSTR);
    }
}
__device__ __forceinline__ unsigned poll_keys(rsrc_t r, int lane) {
    unsigned f[4];
    sweep4(r, WS_KEYS, lane, f, [](unsigned v) { return v == SENT; });
    return wave_max_u(max(max(f[0], f[1]), max(f[2], f[3])));
}

#define MFMA32(a, b, c) __builtin_amdgcn_mfma_f32_32x32x16_f16(a, b, c, 0, 0, 0)

struct ConvPtr { const char *an, *bn, *a2, *b2, *a3, *b3; };
template <int TILE> struct Steps {
    static constexpr int NN = TILE ? 38 : 35, N2 = TILE ? 24 : 8, N3 = TILE ? 8 : 0, N = NN + N2 + N3;
};
template <int TILE, int S>
__device__ __forceinline__ void conv_ld(const ConvPtr& P, f16x8& A, f16x8& B) {
    using St = Steps<TILE>;
    if constexpr (S < St::NN) {
        constexpr int n = S - 1, nb = n > 33 ? n + 29 : n;
        constexpr int boff = (TILE ? 33 : 17) * 80 + ((nb & 1) ? -((nb + 1) / 2) * 80 + 32 : -(nb / 2) * 80);
        A = cat(trread(P.an + S * 32 + 8), trread(P.an + S * 32));
        B = *(const f16x8*)(P.bn + boff);
    } else if constexpr (S < St::NN + St::N2) {
        constexpr int s = S - St::NN;
        A = cat(trread(P.a2 + s * 128 + 32), trread(P.a2 + s * 128));
        B = *(const f16x8*)(P.b2 + (St::N2 - 1 - s) * 32);
    } else {
        constexpr int s = S - St::NN - St::N2;
        A = cat(trread(P.a3 + s * 256), trread(P.a3 + s * 256 + 64));
        B = cat(*(const f16x4*)(P.b3 + (7 - s) * 32 + 8), *(const f16x4*)(P.b3 + (7 - s) * 32));
    }
}
constexpr int PF = 4;
template <int TILE, int S>
__device__ __forceinline__ void conv_steps(const ConvPtr& P, f16x8 (&A)[PF], f16x8 (&B)[PF], f32x16& acc,
                                           volatile LAS unsigned* cnt, unsigned& seen ST_ARG) {
    using St = Steps<TILE>;
    if constexpr (S < St::N) {
        acc = MFMA32(A[S % PF], B[S % PF], acc);
        if constexpr (S + PF + 4 == St::NN) seen = *cnt;
        if constexpr (S + PF == St::NN) {
            SB();
            for (int s = 0; s < SPIN_MAX && __builtin_amdgcn_readfirstlane(seen) < 8u; ++s) seen = *cnt;
            asm volatile("" ::: "memory");
        }
        if constexpr (S + PF < St::N) conv_ld<TILE, S + PF>(P, A[S % PF], B[S % PF]);
        SB();
        conv_steps<TILE, S + 1>(P, A, B, acc, cnt, seen ST_PASS);
    }
}
__device__ __forceinline__ ConvPtr conv_setup(char* smem, int slot, int tile, int lane) {
    const int c = lane & 31, h = lane >> 5, q = (lane >> 2) & 3;
    const int G = ((lane >> 4) & 1) * 4 + (lane & 3);
    const int tr = 16 * (G & 1) + 4 * (G >> 1);
    const char* wsl = smem + slot * WSLOT;
    const char* ks = smem + KB + slot * KSLOT;
    ConvPtr P;
    P.an = ks + q * KCS + 2 * (tr - 8 * h + 8);
    P.bn = smem + UB + slot * USLOT + UOFF + (c - (tile ? 1 : 17)) * 80 + 16 * h;
    P.a2 = wsl + (q & 1) * WSTR + 2 * (36 + 32 * h + tr - 4 * (q + 1) - 16);
    P.b2 = ks + T2 + (1 + c - h) * 16;
    P.a3 = wsl + (2 + (q & 1)) * WSTR + 2 * (36 + 64 * h + tr - 8 * (q + 1));
    P.b3 = ks + T3 + (c - 2 * h - 29) * 8;
    return P;
}
template <int TILE>
__device__ __forceinline__ void conv_tile(char* smem, const ConvPtr& P, int slot, unsigned yo, rsrc_t wsr ST_ARG) {
    f16x8 A[PF], B[PF];
    conv_ld<TILE, 0>(P, A[0], B[0]); conv_ld<TILE, 1>(P, A[1], B[1]);
    conv_ld<TILE, 2>(P, A[2], B[2]); conv_ld<TILE, 3>(P, A[3], B[3]);
    const float sc = *(const float*)(smem + MISC + 96 + slot * 4);
    f32x16 acc;
#pragma unroll
    for (int i = 0; i < 16; ++i) acc[i] = 0.f;
    SB();
    unsigned seen = 0;
    conv_steps<TILE, 0>(P, A, B, acc, (volatile LAS unsigned*)(smem + MISC + 48), seen ST_PASS);
#pragma unroll
    for (int g = 0; g < 2; ++g) {
        u32x4 o;
#pragma unroll
        for (int e = 0; e < 4; ++e) o[e] = pkbf((f32x2){acc[8 * g + 2 * e], acc[8 * g + 2 * e + 1]} * sc);
        st16(wsr, yo + g * 1024, o);
    }
}

template <bool FULL>
__device__ __forceinline__ void conv_windows(char* smem, int slot, int i, int lane) {
    const char* op = smem + UB + slot * USLOT + UOFF + (lane >> 2) * 80 + (lane & 3) * 16 + i * 1280;
    const u32x4 own = *(const u32x4*)op;
    const u32x4 pv = *(const u32x4*)(op - ((lane & 3) ? 16 : 32));
    unsigned w[8] = {pv.x, pv.y, pv.z, pv.w, own.x, own.y, own.z, own.w};
    unsigned a[7];
#pragma unroll
    for (int k = 0; k < 7; ++k) a[k] = __builtin_amdgcn_alignbit(w[k + 1], w[k], 16);
    u32x4 w3, w5, w7, w9;
#pragma unroll
    for (int k = 0; k < 4; ++k) {
        w3[k] = padd(w[2 + k], padd(a[1 + k], a[2 + k]));
        w5[k] = padd(w3[k], padd(w[1 + k], w[3 + k]));
        if (FULL) {
            w7[k] = padd(w5[k], padd(a[k], a[3 + k]));
            w9[k] = padd(w7[k], padd(w[k], w[4 + k]));
        }
    }
    char* wp = smem + slot * WSLOT + 64 + lane * 16 + i * 1024;
    *(u32x4*)(wp) = w3;
    *(u32x4*)(wp + WSTR) = w5;
    if (FULL) {
        *(u32x4*)(wp + 2 * WSTR) = w7;
        *(u32x4*)(wp + 3 * WSTR) = w9;
    }
}

#define SLP_FLAG 6
#define SLP_F2 16
#define SLP_U 12
#define SLP_U2 2
#define FIN_DLY 125
#define SLP_Y -1
#define TOUCH_AT 0
#define TSPLIT 1
#define CPF_N 32
#define SLP_FIN 28
#define SLP_FK 24
#define SLP_CLR 12
#define SLP_KEYS 16
#define SLP_W 12
__device__ __forceinline__ void lds_add1(char* q) {
    const unsigned a = (unsigned)(size_t)(LAS char*)q, one = 1u;
    asm volatile("ds_add_u32 %0, %1" :: "v"(a), "v"(one) : "memory");
}

__device__ __forceinline__ void lds_wait(char* q, unsigned n) {
    for (int s = 0; s < SPIN_MAX; ++s) {
        if (__builtin_amdgcn_readfirstlane(*(volatile LAS unsigned*)q) >= n) break;
        __builtin_amdgcn_s_sleep(1);
    }
    asm volatile("" ::: "memory");
}
#define LGKM0() asm volatile("s_waitcnt lgkmcnt(0)" ::: "memory")
#define RB2(v) rbf2(v)

extern "C" __global__ void __launch_bounds__(1024, 4)
fused_kernel(const u16* __restrict__ x, const u16* __restrict__ logA, const u16* __restrict__ Aim,
             const u16* __restrict__ Cre, const u16* __restrict__ Cim, const u16* __restrict__ logdt,
             const u16* __restrict__ dskip, const u16* __restrict__ wr, const u16* __restrict__ wg,
             const u16* __restrict__ wu, const u16* __restrict__ wd, u16* out, char* ws) {
    extern __shared__ __attribute__((aligned(16))) char smem[];
    asm volatile("" :: "s"(x), "s"(logA), "s"(Aim), "s"(Cre), "s"(Cim), "s"(logdt), "s"(dskip), "s"(wr), "s"(wg), "s"(wu),
                 "s"(wd), "s"(out), "s"(ws));
    const int tid = threadIdx.x, lane = tid & 63;
    const int wave = __builtin_amdgcn_readfirstlane(tid >> 6);
    const int p = (blockIdx.x & 7) * 32 + (blockIdx.x >> 3);
    const int isB = p & 1;
    const int d0 = 5 * (p >> 1) + 2 * isB;
    const int fs = (p >> 5) * 14 + (p & 31);
    const bool ffn = (p & 31) < 14 && fs < 108;
    const rsrc_t wsr = __builtin_amdgcn_make_buffer_rsrc(ws, 0, WS_END, 0x00020000);
    const u32x4 s4 = {SENT, SENT, SENT, SENT};
    STAMP_DECL; STAMP(0);

    auto ld5 = [&](const u16* q_, unsigned (&o)[5]) {
        u32x2 a = *(const u32x2*)(q_ + lane * 4), b = *(const u32x2*)(q_ + 256 + lane * 4);
        o[0] = a.x; o[1] = a.y; o[2] = b.x; o[3] = b.y; o[4] = *(const unsigned*)(q_ + 512 + lane * 2);
    };
    auto chof = [&](int s) { return isB ? d0 + (s == 2 ? 0 : s + 1) : d0 + s; };
    const int row = p * 8 + (wave & 7);
    const int sub = wave & 3;
    unsigned xr[5];
    unsigned tag = 0, cpf = 0, cpf2 = 0;
    u32x4 tg = {0u, 0u, 0u, 0u};
    float mb1 = 0.f, rs1 = 0.f;
    if (wave >= 8) {
        const int slot = wave >= 12 ? 1 + (sub >> 1) : 0;
        const bool doA = wave >= 10, doB = wave >= 12 || wave < 10;
        const int dch = chof(slot);
        const unsigned ldt_ = ((const unsigned*)logdt)[dch >> 1];
        const unsigned la_ = ((const unsigned*)(logA + dch * NM))[lane];
        const unsigned ai_ = ((const unsigned*)(Aim + dch * NM))[lane];
        const unsigned c0 = ((const unsigned*)(Cre + dch * NM))[lane];
        const unsigned c1 = ((const unsigned*)(Cim + dch * NM))[lane];
        BAR_NOWAIT();
        if (wave >= 12) __builtin_amdgcn_s_setprio(2);
        const unsigned sh = (wave & 1) ? 0u : 16u;
        auto pick = [&](unsigned w) { return __uint_as_float((w << sh) & 0xffff0000u); };
        const float dt = rbf(exp_acc(__uint_as_float((dch & 1) ? ldt_ & 0xffff0000u : ldt_ << 16)));
        const float zr = -rbf(exp_acc(pick(la_))) * dt;
        const float zi = pick(ai_) * dt;
        const float zl = zr * 1.44269504088896341f;
        const float wh = zi * 0.15915494309189535f;
        const float wl = fmaf(zi, 6.4206383e-09f, fmaf(zi, 0.15915494309189535f, -wh));
        STAMP(1);
        const f32x2 e8 = cexp_p(zl, wh, wl, 8.f);
        const f32x2 e128 = cexp_p(zl, wh, wl, 128.f);
        const f32x2 e256 = cmul(e128, e128), e384 = cmul(e128, e256), e512 = cmul(e256, e256);
        char* tb = smem + slot * TABSZ + ((wave & 1) * 64 + lane) * MSTR;
        auto put8 = [&](int grp, const f32x2 (&e)[8]) {
            u32x4 re = {pkh(e[0].x, e[1].x), pkh(e[2].x, e[3].x), pkh(e[4].x, e[5].x), pkh(e[6].x, e[7].x)};
            u32x4 im = {pkh(e[0].y, e[1].y), pkh(e[2].y, e[3].y), pkh(e[4].y, e[5].y), pkh(e[6].y, e[7].y)};
            *(u32x4*)(tb + grp * 16) = re;
            *(u32x4*)(tb + 128 + grp * 16) = im;
        };
        if (doA) {
            f32x2 e[8], g[8];
            e[0] = (f32x2){1.f, 0.f};
            e[1] = cexp_p(zl, wh, wl, 1.f);
            e[2] = cmul(e[1], e[1]); e[4] = cmul(e[2], e[2]);
            e[3] = cmul(e[1], e[2]); e[5] = cmul(e[1], e[4]); e[6] = cmul(e[2], e[4]); e[7] = cmul(e[3], e[4]);
            put8(0, e);
            g[0] = e128;
#pragma unroll
            for (int r = 1; r < 8; ++r) g[r] = cmul(e[r], e128);
            put8(1, g);
            g[0] = cmul(e[2], e256); g[1] = cmul(e[4], e256); g[2] = cmul(e[6], e256); g[3] = cmul(e8, e256);
            g[4] = cmul(e[2], e384); g[5] = cmul(e[4], e384); g[6] = cmul(e[6], e384); g[7] = cmul(e8, e384);
            put8(2, g);
            g[0] = cmul(e512, e[4]); g[1] = cmul(e512, e8);
            g[2] = cmul(g[0], e128); g[3] = cmul(g[1], e128);
            g[4] = cmul(g[0], e256); g[5] = cmul(g[1], e256);
            g[6] = cmul(g[0], e384); g[7] = cmul(g[1], e384);
            put8(3, g);
        }
        if (doB) {
            const unsigned m0 = c0 & 0x7fff7fffu, m1 = c1 & 0x7fff7fffu;
            const unsigned mx = wave_max_u(max(max(m0 >> 16, m0 & 0xffffu), max(m1 >> 16, m1 & 0xffffu)));
            const int ex = min(max((int)(mx >> 7), 8), 246);
            const float up = __uint_as_float((unsigned)(260 - ex) << 23);
            if (lane == 0) *(float*)(smem + MISC + 96 + slot * 4) = 2.f * dt * __uint_as_float((unsigned)(ex - 6) << 23);
            const f32x2 c8 = {e8.x, -e8.y};
            const f32x2 c16 = cmul(c8, c8), c32 = cmul(c16, c16), c64 = cmul(c32, c32);
            const f32x2 e1024 = cmul(e512, e512);
            f32x2 b[8], g[8];
            g[0] = cmul(e1024, e8); g[1] = cmul(g[0], e128); g[2] = cmul(g[0], e256); g[3] = cmul(g[0], e384);
#pragma unroll
            for (int s = 0; s < 4; ++s) g[4 + s] = cmul(g[s], e512);
            put8(4, g);
            *(u32x2*)(tb + 80) = (u32x2){pkh(e256.x, e512.x), pkh(e1024.x, 0.f)};
            *(u32x2*)(tb + 128 + 80) = (u32x2){pkh(e256.y, e512.y), pkh(e1024.y, 0.f)};
            b[0] = (f32x2){pick(c0) * up, -pick(c1) * up};
            b[1] = cmul(b[0], c8); b[2] = cmul(b[0], c16); b[3] = cmul(b[1], c16);
#pragma unroll
            for (int j = 0; j < 4; ++j) b[4 + j] = cmul(b[j], c32);
            put8(6, b);
#pragma unroll
            for (int j = 0; j < 8; ++j) g[j] = cmul(b[j], c64);
            put8(7, g);
        }
        LGKM0();
        if (lane == 0) lds_add1(smem + MISC + 64 + slot * 4);
        __builtin_amdgcn_s_setprio(0);
        STAMP(4);
        } else {
        ld5(x + (size_t)row * D, xr);
        tag = ld4(wsr, WS_EPOCH);
        {
            u64 pc; unsigned off, len;
            asm volatile("s_getpc_b64 %0\n.Lfk_pc:" : "=s"(pc));
            asm volatile("s_mov_b32 %0, .Lfk_pc-fused_kernel\n\ts_mov_b32 %1, .Lfk_end-fused_kernel" : "=s"(off), "=s"(len));
            const rsrc_t cr = __builtin_amdgcn_make_buffer_rsrc((void*)(pc - off), 0, len, 0x00020000);
            cpf = __builtin_amdgcn_raw_buffer_load_b32(cr, tid < CPF_N ? ((p & (256 / CPF_N - 1)) * CPF_N + tid) * 128 : 1 << 20, 0, 0);
        }
        if (lane < 4) *(u32x4*)(smem + MISC + 32 + lane * 16) = (u32x4){0u, 0u, 0u, 0u};
        char* tp = smem + TRB + wave * RSTR + lane * 4;
        const char* tp2 = smem + TRB + ((lane >> 2) & 3) * RSTR + ((lane >> 4) * 16 + (lane & 3) * 4) * 2 + wave * 128;
        const unsigned uso = WS_UT + (p * D + tid) * 16;
        BAR();
        {
            const int c1_ = 512 + wave * 16 + lane;
            const unsigned yb = WS_Y + d0 * 4096;
            st16(wsr, yb + (tid + ((isB && tid >= 128) ? 128 : 0)) * 16, s4);
            st16(wsr, lane < 16 ? yb + (c1_ + 128) * 16 : WS_END, s4);
        }
        const u16* tsrc;
        {
            const int li = (p & 31) * 80 + wave * 10 + min(lane, 9);
            tsrc = wg + (size_t)(li >> 2) * F + min(((7 * (p >> 5)) >> 1) + (li & 3), 26) * 64;
        }
        SB();
        __builtin_amdgcn_s_setprio(3);
        f32x2 v[5];
#pragma unroll
        for (int i = 0; i < 5; ++i) v[i] = unpk(xr[i]);
        asm volatile("" : "+v"(v[4]), "+v"(tsrc));
        cpf2 = *(const __attribute__((address_space(1))) unsigned*)tsrc;
        SB();
        STAMP(1);
        row_stats(v, mb1, rs1);
        unsigned uh[5];
#pragma unroll
        for (int i = 0; i < 5; ++i) {
            const f32x2 u = RB2(RB2(v[i] - mb1) * rs1);
            uh[i] = pkh(u.x, u.y);
        }
        *(u32x2*)(tp + lane * 4) = (u32x2){uh[0], uh[1]};
        *(u32x2*)(tp + 512 + lane * 4) = (u32x2){uh[2], uh[3]};
        *(unsigned*)(tp + 1024) = uh[4];
        tag = __builtin_amdgcn_readfirstlane(tag) + 1u;
        asm volatile("s_waitcnt vmcnt(1) lgkmcnt(0)" : "+s"(tag) :: "memory");
        if (lane == 0) lds_add1(smem + MISC + 52);
#pragma unroll
        for (int i = 0; i < 4; ++i) {
            const unsigned f6 = i ? (tag >> (6 * i)) + tag : tag;
            tg[i] = (f6 & 7u) | ((f6 & 56u) << 13);
        }
        if (wave < 4) {
            lds_wait(smem + MISC + 52, 8u);
            STAMP(2);
#pragma unroll
            for (int i = 0; i < 3; ++i) {
                if (i < 2 || wave < 2) {
                    u32x4 o = __builtin_bit_cast(u32x4, cat(trread(tp2 + i * 512), trread(tp2 + i * 512 + 4 * RSTR)));
                    o = (o & 0xfff8fff8u) | tg;
                    st16(wsr, uso + i * 4096, o);
                }
            }
        }
        __builtin_amdgcn_s_setprio(0);
        STAMP(3);
    }
    if (wave >= 8 || wave == 3) {
        const int ts = wave == 3 ? 2 : (wave == 11 ? 1 : (wave >= 12 ? 1 + (sub >> 1) : 0));
        const int tt = wave >= 12 ? (sub & 1) : (sub == 3 ? 2 : sub);
        char* ks = smem + KB + ts * KSLOT;
        const int qq = lane >> 4, q = (lane >> 2) & 3, pp = lane & 3, j = lane & 15;
        const char* ap = smem + ts * TABSZ + (4 * qq + 2 * (q >> 1)) * MSTR + (q & 1) * 128 + pp * 8;
        const char* bp = ap + (tt == 2 ? 64 : 96);
        ap += tt == 2 ? 96 : tt * 32;
        lds_wait(smem + MISC + 64 + ts * 4, ts ? 6u : 8u);
        STAMP(5);
        f16x8 av[8], bv[8];
#pragma unroll
        for (int k = 0; k < 8; ++k) {
            av[k] = cat(trread(ap + k * 16 * MSTR), trread(ap + k * 16 * MSTR + MSTR));
            bv[k] = cat(trread(bp + k * 16 * MSTR), trread(bp + k * 16 * MSTR + MSTR));
        }
        SB();
        f32x4 acc = {0.f, 0.f, 0.f, 0.f}, acc1 = {0.f, 0.f, 0.f, 0.f};
#pragma unroll
        for (int k = 0; k < 4; ++k) {
            acc = __builtin_amdgcn_mfma_f32_16x16x32_f16(av[2 * k], bv[2 * k], acc, 0, 0, 0);
            acc1 = __builtin_amdgcn_mfma_f32_16x16x32_f16(av[2 * k + 1], bv[2 * k + 1], acc1, 0, 0, 0);
        }
        acc += acc1;
        const unsigned p01 = pkh(acc[0], acc[1]), p23 = pkh(acc[2], acc[3]), p12 = pkh(acc[1], acc[2]);
        if (tt == 0) {
            char* o = ks + 2 * (28 + 4 * (qq & 1) + 128 * (qq >> 1) + 8 * j);
            *(u32x2*)o = (u32x2){p01, p23};
            *(unsigned*)(o + 2 * KCS + 4) = p01; *(unsigned*)(o + 2 * KCS + 8) = p23;
            *(u16*)(o + KCS + 2) = (u16)p01; *(unsigned*)(o + KCS + 4) = p12; *(u16*)(o + KCS + 8) = (u16)(p23 >> 16);
            *(u16*)(o + 3 * KCS + 6) = (u16)p01; *(unsigned*)(o + 3 * KCS + 8) = p12; *(u16*)(o + 3 * KCS + 12) = (u16)(p23 >> 16);
        } else if (tt == 1) {
            if (qq < 2) {
                const unsigned p11 = pkh(acc[1], acc[1]), p33 = pkh(acc[3], acc[3]);
                char* o = ks + 2 * (28 + 258 + 128 * qq + 8 * j);
                unsigned* o0 = (unsigned*)o;
                o0[0] = p01; o0[1] = p11; o0[2] = p23; o0[3] = p33;
                *(u32x4*)(o + 2 * KCS + 4) = (u32x4){p01, p11, p23, p33};
                *(u16*)(o + KCS + 2) = (u16)p01; *(u32x2*)(o + KCS + 4) = (u32x2){p11, p12};
                *(unsigned*)(o + KCS + 12) = p33; *(u16*)(o + KCS + 16) = (u16)p33;
                *(u16*)(o + 3 * KCS + 6) = (u16)p01; *(unsigned*)(o + 3 * KCS + 8) = p11;
                *(u32x2*)(o + 3 * KCS + 12) = (u32x2){p12, p33}; *(u16*)(o + 3 * KCS + 20) = (u16)p33;
            } else {
                unsigned* t2 = (unsigned*)(ks + T2 + 31 * 16) + 32 * (qq - 2) + j;
                t2[0] = p01; t2[16] = p23;
            }
        } else {
            if (j < 8) *(u32x2*)(ks + T3 + (16 * j + 4 * qq) * 2) = (u32x2){p01, p23};
            const unsigned s256 = __builtin_amdgcn_readlane((int)p01, 8), s512 = __builtin_amdgcn_readlane((int)p01, 9),
                           s1024 = __builtin_amdgcn_readlane((int)p01, 10);
            const int w = lane >> 2, cq = lane & 3;
            const int lg = w < 2 ? 256 + w : (w == 2 ? 514 : 560 + w);
            const unsigned sv = w < 2 ? s256 : (w == 2 ? s512 : s1024);
            if (w < 5) *(u16*)(ks + cq * KCS + 2 * (lg + cq + 28)) = (u16)sv;
        }
        STAMP(6);
    } else if (wave < 3) {
        if (wave == 2) {
            __builtin_amdgcn_s_sleep(SLP_CLR);
#pragma unroll
            for (int i = 0; i < 5; ++i) if (i < 3 || ffn) st16(wsr, WS_REC + p * 5120 + (i * 64 + lane) * 16, s4);
            if (lane == 0) st4(wsr, WS_KEYS + p * FSTR, SENT);
            DRAIN();
        }
        STAMP(6);
    } else {
        {
            const u32x4 z = {0u, 0u, 0u, 0u};
            const int zi = tid - 256;
#pragma unroll
            for (int i = 0; i < 6; ++i) if (i < 5 || zi < 3 * KSLOT / 16 - 1280) *(u32x4*)(smem + KB + (i * 256 + zi) * 16) = z;
            if (zi < 255) { const int s_ = zi / 85; *(u32x4*)(smem + UB + s_ * USLOT + (zi - 85 * s_) * 16) = z; }
            LGKM0();
            if (lane < 3) lds_add1(smem + MISC + 64 + lane * 4);
        }
        unsigned uo[3], lo[3];
#pragma unroll
        for (int i = 0; i < 3; ++i) {
            const int idx = i * 64 + lane, g0 = idx / 3, uc = idx - 3 * g0, ug = (wave - 4) * 64 + g0;
            const int us = isB ? (uc == 0 ? 2 : uc - 1) : uc;
            uo[i] = WS_UT + (ug * D + d0 + uc) * 16;
            lo[i] = UB + us * USLOT + UOFF + (ug >> 2) * 80 + (ug & 3) * 16;
        }
        __builtin_amdgcn_s_sleep(SLP_U);
        STAMP(4);
        u32x4 uv[3];
        for (int s = 0; s < SPIN_MAX; ++s) {
            asm volatile("" ::: "memory");
#pragma unroll
            for (int i = 0; i < 3; ++i) uv[i] = ld16(wsr, uo[i]);
            unsigned bad = 0;
#pragma unroll
            for (int i = 0; i < 3; ++i) {
                const u32x4 t = uv[i] ^ tg;
                bad |= t.x | t.y | t.z | t.w;
            }
            if (!__any((bad & 0x00070007u) != 0u)) break;
            __builtin_amdgcn_s_sleep(SLP_U2);
        }
#pragma unroll
        for (int i = 0; i < 3; ++i) *(u32x4*)(smem + lo[i]) = uv[i] & 0xfff8fff8u;
        STAMP(6);
    }
    const int cslot = wave == 11 ? 0 : (wave == 15 ? 1 : wave - 8);
    const int ctile = (sub == 3 || (wave == 10 && isB)) ? 0 : 1;
    const ConvPtr CP = conv_setup(smem, cslot, ctile, lane);
    const unsigned cyo = WS_Y + (chof(cslot) * 256 + ctile * 128 + lane) * 16;
    STAMP(7);
    BAR();
    STAMP(8);
    unsigned wdp[16], er[5], wv[5];
    if (wave < 8) {
        ld5(dskip, er);
        ld5(wr, wv);
        if (tid == 448 && p == 0) st4(wsr, WS_EPOCH, tag);
        if (wave < 6) conv_windows<true>(smem, wave >> 1, wave & 1, lane);
        else {
            conv_windows<false>(smem, wave - 6, 2, lane);
            if (wave == 6) conv_windows<false>(smem, 2, 2, lane);
            else if (lane < 48) *(u32x4*)(smem + (lane >> 2) * WSTR + (lane & 3) * 16) = (u32x4){0u, 0u, 0u, 0u};
        }
        LGKM0();
        if (lane == 0) lds_add1(smem + MISC + 48);
        STAMP(9);
#pragma unroll
        for (int i = 0; i < 5; ++i) er[i] = pkbf(RB2(RB2(unpk(xr[i]) - mb1) * rs1) * unpk(er[i]));
        if (ffn && wave < 5) {
            const unsigned* wp = (const unsigned*)(wd + (size_t)fs * 16 * D) + tid;
#pragma unroll
            for (int i = 0; i < 16; ++i) wdp[i] = wp[i * (D / 2)];
        }
        STAMP(5);
    } else if (wave < 12 || wave == 15) {
        if (wave < 12) STAMPC(2);
        if (ctile) conv_tile<1>(smem, CP, cslot, cyo, wsr ST_PASS);
        else conv_tile<0>(smem, CP, cslot, cyo, wsr ST_PASS);
        if (wave < 12) STAMPC(3);
        STAMP(9);
        __builtin_amdgcn_s_setprio(0);
        DRAIN();
    } else if (ffn) {
        u32x4 wq[14];
        const int xi = tid - 768;
#pragma unroll
        for (int i = 0; i < 14; ++i) {
            const int ci = min(i * 192 + xi, 2559), r_ = ci >> 1;
            const u16* wsrc = r_ < D ? wg + (size_t)r_ * F : wu + (size_t)(r_ - D) * F;
            if (i < 13 || wave == 12) wq[i] = *(const u32x4*)(wsrc + fs * 16 + (ci & 1) * 8);
        }
        SB();
#pragma unroll
        for (int i = 0; i < 14; ++i) if (i < 13 || wave == 12) *(u32x4*)(smem + WG + (i * 192 + xi) * 16) = wq[i];
        STAMP(9);
    }
    else if ((p & 31) >= 14) {
        const int li = min(((p & 31) - 14) * 143 + min(tid - 768, 142), 2559);
        cpf2 = *(const unsigned*)(wu + (size_t)(li >> 2) * F + min(((7 * (p >> 5)) >> 1) + (li & 3), 26) * 64);
    }
    asm volatile("" :: "v"(cpf), "v"(cpf2));
    BAR();
    STAMP(10);
    if (wave < 10) {
        const unsigned yoff = WS_Y + (tid * 256 + (p >> 7) * 128 + (p & 1) * 64 + ((p >> 1) & 1) * 32 + ((p >> 2) & 31)) * 16;
        const u32x4 yv = poll16(wsr, yoff, ld16(wsr, yoff));
        u16* yp = (u16*)(smem + TRB) + tid;
#pragma unroll
        for (int i = 0; i < 4; ++i) {
            yp[(2 * i) * D] = (u16)yv[i];
            yp[(2 * i + 1) * D] = (u16)(yv[i] >> 16);
        }
    }
    STAMP(11);
    BAR();
    unsigned op[5];
    bool plain_out = false;
    if (wave < 8) {
        unsigned yr[5], hp[5], zp[5];
        ld5((const u16*)(smem + TRB) + wave * D, yr);
        f32x2 hv[5];
#pragma unroll
        for (int i = 0; i < 5; ++i) {
            hp[i] = pkbf(unpk(xr[i]) + RB2(unpk(yr[i]) + unpk(er[i])));
            hv[i] = unpk(hp[i]);
        }
        float mb, rs;
        row_stats(hv, mb, rs);
        float ra = 0.f;
#pragma unroll
        for (int i = 0; i < 5; ++i) {
            zp[i] = pkbf(RB2(hv[i] - mb) * rs);
            ra = dot2bf(zp[i], wv[i], ra);
        }
        const float r = wave_sum_u(ra);
        unsigned rb = f2bf(r);
        rb = (rb & 0x8000u) ? (~rb & 0xffffu) : (rb | 0x8000u);
        const unsigned key = (rb << 16) | (unsigned)(2047 - row);
        if (lane == 0) *(unsigned*)(smem + MISC + wave * 4) = key;
        BAR();
        STAMP(12);
        const u32x4 k0 = *(const u32x4*)(smem + MISC), k1 = *(const u32x4*)(smem + MISC + 16);
        const unsigned best = max(max(max(k0.x, k0.y), max(k0.z, k0.w)), max(max(k1.x, k1.y), max(k1.z, k1.w)));
        plain_out = __builtin_amdgcn_readfirstlane(best) != key;
        if (!plain_out) {
            const unsigned rec = WS_REC + p * 5120;
            if (lane == 0) st4(wsr, WS_KEYS + p * FSTR, key);
            st8(wsr, rec + lane * 8, (u32x2){zp[0], zp[1]});
            st8(wsr, rec + 512 + lane * 8, (u32x2){zp[2], zp[3]});
            st4(wsr, rec + 1024 + lane * 4, zp[4]);
        }
#pragma unroll
        for (int i = 0; i < 5; ++i) op[i] = pkbf(hv[i] + unpk(zp[i]));
        if (!plain_out) {
            const unsigned rec = WS_REC + p * 5120;
            const rsrc_t orr = __builtin_amdgcn_make_buffer_rsrc(out + (size_t)row * D, 0, D * 2, 0x00020000);
            st8(orr, lane * 8, (u32x2){op[0], op[1]});
            st8(orr, 512 + lane * 8, (u32x2){op[2], op[3]});
            st4(orr, 1024 + lane * 4, op[4]);
            DRAIN();
            st8(wsr, rec + 1280 + lane * 8, (u32x2){hp[0], hp[1]});
            st8(wsr, rec + 1280 + 512 + lane * 8, (u32x2){hp[2], hp[3]});
            st4(wsr, rec + 1280 + 1024 + lane * 4, hp[4]);
        }
    } else {
        BAR();
        STAMP(12);
    }
    if (ffn) {
        f16x8 bw[4];
        const int mat = wave >= 5, kgp = wave - 5 * mat;
        if (wave < 10) {
            const char* bp = smem + WG + mat * 20480 + (kgp * 128 + (lane >> 4) * 8 + ((lane >> 2) & 3)) * 32 + (lane & 3) * 8;
#pragma unroll
            for (int k = 0; k < 4; ++k) bw[k] = cat(trread(bp + k * 1024), trread(bp + k * 1024 + 128));
        } else if (wave == 15) {
            __builtin_amdgcn_s_sleep(SLP_KEYS);
            const unsigned bk = poll_keys(wsr, lane);
            if (lane == 0) *(unsigned*)(smem + MISC + 32) = bk;
        }
        unsigned wq[16];
        if (wave < 5) {
#pragma unroll
            for (int i = 0; i < 8; ++i) {
                wq[2 * i] = __builtin_amdgcn_perm(wdp[2 * i + 1], wdp[2 * i], 0x05040100u);
                wq[2 * i + 1] = __builtin_amdgcn_perm(wdp[2 * i + 1], wdp[2 * i], 0x07060302u);
            }
        }
        BAR();
        STAMP(13);
        if (wave < 10) {
            const unsigned bk = *(const unsigned*)(smem + MISC + 32);
            const unsigned xo = WS_REC + ((2047u - (bk & 2047u)) >> 3) * 5120 + (kgp * 128 + (lane >> 4) * 8) * 2;
            u32x4 xa[4];
#pragma unroll
            for (int k = 0; k < 4; ++k) xa[k] = ld16(wsr, xo + k * 64);
#pragma unroll
            for (int k = 0; k < 4; ++k) xa[k] = poll16(wsr, xo + k * 64, xa[k]);
            f32x4 acc = {0.f, 0.f, 0.f, 0.f};
#pragma unroll
            for (int k = 0; k < 4; ++k)
                acc = __builtin_amdgcn_mfma_f32_16x16x32_bf16(__builtin_bit_cast(bf16x8, xa[k]), __builtin_bit_cast(bf16x8, bw[k]), acc, 0, 0, 0);
            if (lane < 16) *(float*)(smem + MISC + 128 + (wave * 16 + lane) * 4) = acc[0];
        }
        BAR();
        STAMP(14);
        if (wave < 5) {
            const float* pr = (const float*)(smem + MISC + 128) + (lane & 15);
            const float g = rbf(pr[0] + pr[16] + pr[32] + pr[48] + pr[64]);
            const float u = rbf(pr[80] + pr[96] + pr[112] + pr[128] + pr[144]);
            const unsigned a = f2bf(rbf(g * sigmoid_f(g)) * u);
            const unsigned ap = a | (dppu<0xB1>(a) << 16);
            float y0 = 0.f, y1 = 0.f;
#pragma unroll
            for (int i = 0; i < 8; ++i) {
                const unsigned s = (unsigned)__builtin_amdgcn_readlane((int)ap, 2 * i);
                y0 = dot2bf(wq[2 * i], s, y0);
                y1 = dot2bf(wq[2 * i + 1], s, y1);
            }
            st8(wsr, WS_REC + p * 5120 + 2560 + tid * 8, (u32x2){__float_as_uint(y0), __float_as_uint(y1)});
            __builtin_amdgcn_s_sleep(4);
        } else {
            __builtin_amdgcn_s_sleep(14);
        }
        STAMP(15);
    } else if (wave < 8) {
        for (int i = 0; i <= wave; ++i) __builtin_amdgcn_s_sleep(6);
    } else if ((p & 31) >= 16 && (p & 31) < 26 && wave >= 14) {
        const int cq = 2 * ((p >> 5) * 10 + (p & 31) - 16) + (wave - 14);
        __builtin_amdgcn_s_sleep(SLP_KEYS);
        const unsigned bk = poll_keys(wsr, lane);
        const u64 tk = __builtin_amdgcn_s_memrealtime();
        const int wrow = 2047 - (int)(bk & 2047u);
        const unsigned zo = WS_REC + (wrow >> 3) * 5120 + cq * 8 + (lane & 1) * 1280;
        u32x2 zh = ld8(wsr, zo);
        const int f0 = min(lane, 53), f1 = f0 + 54;
        const unsigned po0 = WS_REC + ((f0 / 14) * 32 + f0 % 14) * 5120 + 2560 + cq * 16;
        const unsigned po1 = WS_REC + ((f1 / 14) * 32 + f1 % 14) * 5120 + 2560 + cq * 16;
        for (int s = 0; s < SPIN_MAX; ++s) {
            const bool pend = max(zh.x, zh.y) == SENT;
            if (!__any(pend)) break;
            __builtin_amdgcn_s_sleep(2);
            asm volatile("" ::: "memory");
            if (pend) zh = ld8(wsr, zo);
        }
        unsigned rb = bk >> 16;
        rb = (rb & 0x8000u) ? (rb & 0x7fffu) : (~rb & 0xffffu);
        const float rsel = rbf(sigmoid_f(bf2f(rb)));
        const int ce = ((lane >> 5) & 1) | ((lane >> 3) & 2);
        const unsigned zq = __builtin_amdgcn_readlane((int)zh.x, 0), zq1 = __builtin_amdgcn_readlane((int)zh.y, 0);
        const unsigned hq = __builtin_amdgcn_readlane((int)zh.x, 1), hq1 = __builtin_amdgcn_readlane((int)zh.y, 1);
        const unsigned zs = (ce & 2) ? zq1 : zq, hs = (ce & 2) ? hq1 : hq;
        const float zf = __uint_as_float((ce & 1) ? zs & 0xffff0000u : zs << 16), hf = __uint_as_float((ce & 1) ? hs & 0xffff0000u : hs << 16);
        u16* op_ = out + (size_t)wrow * D + cq * 4 + ce;
        for (int s = 0; s < 200 && __builtin_amdgcn_s_memrealtime() - tk < FIN_DLY; ++s) __builtin_amdgcn_s_sleep(1);
        STAMP(13);
        u32x4 v0 = ld16(wsr, po0), v1 = ld16(wsr, po1);
        v0 = poll16(wsr, po0, v0);
        v1 = poll16(wsr, po1, v1);
        STAMP(14);
        f32x4 sv = __builtin_bit_cast(f32x4, v0) + __builtin_bit_cast(f32x4, v1);
        if (lane >= 54) sv = (f32x4){0.f, 0.f, 0.f, 0.f};
        auto s01 = __builtin_amdgcn_permlane32_swap(__float_as_uint(sv[0]), __float_as_uint(sv[1]), false, false);
        auto s23 = __builtin_amdgcn_permlane32_swap(__float_as_uint(sv[2]), __float_as_uint(sv[3]), false, false);
        const float a01 = __uint_as_float(s01[0]) + __uint_as_float(s01[1]), a23 = __uint_as_float(s23[0]) + __uint_as_float(s23[1]);
        auto sq = __builtin_amdgcn_permlane16_swap(__float_as_uint(a01), __float_as_uint(a23), false, false);
        const float tot = row16_sum(__uint_as_float(sq[0]) + __uint_as_float(sq[1]));
        const float yv = rbf(rbf(tot) * rsel);
        if ((lane & 15) == 0) *op_ = (u16)f2bf(hf + rbf(zf + yv));
        STAMP(11);
    }
    if (plain_out) {
        u16* q_ = out + (size_t)row * D;
        *(u32x2*)(q_ + lane * 4) = (u32x2){op[0], op[1]};
        *(u32x2*)(q_ + 256 + lane * 4) = (u32x2){op[2], op[3]};
        *(unsigned*)(q_ + 512 + lane * 2) = op[4];
    }
    STAMP_END();
    asm volatile(".Lfk_end:");
}

extern "C" void kernel_launch(void* const* d_in, const int* in_sizes, int n_in, void* d_out, int out_size,
                              void* d_ws, size_t ws_size, hipStream_t stream) {
    (void)hipFuncSetAttribute((const void*)fused_kernel, hipFuncAttributeMaxDynamicSharedMemorySize, LDS_BYTES);
    fused_kernel<<<256, 1024, LDS_BYTES, stream>>>(
        (const u16*)d_in[0], (const u16*)d_in[1], (const u16*)d_in[2], (const u16*)d_in[3], (const u16*)d_in[4],
        (const u16*)d_in[5], (const u16*)d_in[6], (const u16*)d_in[8], (const u16*)d_in[9], (const u16*)d_in[10],
        (const u16*)d_in[11], (u16*)d_out, (char*)d_ws);
}
```
